```python
import torch

def _softmax(x, axis=-1):
    return torch.softmax(x, dim=axis)

def _gelu(x):
    return torch.nn.functional.gelu(x, approximate='tanh')

def _silu(x):
    return torch.nn.functional.silu(x)

def _sigmoid(x):
    return torch.sigmoid(x)

def _layernorm(x, axis=-1, eps=1e-05):
    m = x.mean(dim=axis, keepdim=True)
    v = x.var(dim=axis, keepdim=True, unbiased=False)
    return (x - m) / torch.sqrt(v + eps)

def _scatter_add(tgt, idx, src, axis=0):
    return tgt.clone().index_add_(axis, idx, src)

def _segment_sum(src, seg, n):
    out = torch.zeros((n,) + src.shape[1:], dtype=src.dtype)
    return out.index_add_(0, seg, src)

def _pad(x, pw):
    flat = tuple((v for pair in reversed(pw) for v in pair))
    return torch.nn.functional.pad(x, flat)
kernel_name = 'gated_mix__memory_aug_recurrent_mem_tr__sliding_block__prompt'

def setup_inputs():

    _w = lambda a: (lambda t: t.to(torch.bfloat16) if t.is_floating_point() else t)(torch.as_tensor(a))

    def _A_setup():
        B, S, Dm = (1, 2048, 512)
        H, Dh = (32, 128)
        M, seg = (8, 32)
        seg = min(seg, S)
        S = S // seg * seg
        return {'x': _w((lambda *s: torch.randn(*s, dtype=torch.bfloat16) * 0.05)(B, S, Dm)), 'mem0': _w((lambda *s: torch.randn(*s, dtype=torch.bfloat16) * 1.6)(B, M, Dm)), 'Wq': _w((lambda *s: torch.randn(*s, dtype=torch.bfloat16) * 0.8)(Dm, H, Dh) * Dm ** (-0.5)), 'Wk': _w((lambda *s: torch.randn(*s, dtype=torch.bfloat16) * 0.8)(Dm, H, Dh) * Dm ** (-0.5)), 'Wv': _w((lambda *s: torch.randn(*s, dtype=torch.bfloat16) * 1.6)(Dm, H, Dh) * Dm ** (-0.5)), 'Wo': _w((lambda *s: torch.randn(*s, dtype=torch.bfloat16) * 1.6)(H, Dh, Dm) * (H * Dh) ** (-0.5)), 'seg_len': seg}
    _A_in = _A_setup()

    def _B_setup():
        B, S, D = (1, 2048, 512)
        return {'x': _w((lambda *s: torch.randn(*s, dtype=torch.bfloat16) * 0.05)(B, S, D)), 'wqkv': _w((lambda *s: torch.randn(*s, dtype=torch.bfloat16) * 1.6)(D, 3 * D)), 'wo': _w((lambda *s: torch.randn(*s, dtype=torch.bfloat16) * 0.05)(D, D))}
    _B_in = _B_setup()
    return {'A_x': _A_in['x'], 'A_mem0': _A_in['mem0'], 'A_Wq': _A_in['Wq'], 'A_Wk': _A_in['Wk'], 'A_Wv': _A_in['Wv'], 'A_Wo': _A_in['Wo'], 'A_seg_len': _A_in['seg_len'], 'B_x': _B_in['x'], 'B_wqkv': _B_in['wqkv'], 'B_wo': _B_in['wo'], 'gate_w': _w((lambda *s: torch.randn(*s, dtype=torch.bfloat16) * 0.05)(512) * 16.0)}

def reference(A_x, A_mem0, A_Wq, A_Wk, A_Wv, A_Wo, A_seg_len, B_x, B_wqkv, B_wo, gate_w):

    def _A_ref(x, mem0, Wq, Wk, Wv, Wo, seg_len):
        B, S, Dm = x.shape
        H, Dh = (Wq.shape[1], Wq.shape[2])
        M = mem0.shape[1]
        scale = Dh ** (-0.5)
        n_seg = S // seg_len
        L = M + seg_len
        idx = torch.arange(L)
        mask = (idx[:, None] < M) | (idx[:, None] >= idx[None, :])
        mem = mem0
        outs = []
        for s in range(n_seg):
            xs = x[:, s * seg_len:(s + 1) * seg_len, :].clone()
            h = torch.concatenate([mem, xs], axis=1)
            q = torch.einsum('btd,dhe->bhte', h, Wq)
            k = torch.einsum('btd,dhe->bhte', h, Wk)
            v = torch.einsum('btd,dhe->bhte', h, Wv)
            scores = torch.einsum('bhqe,bhke->bhqk', q, k) * scale
            scores = torch.where(mask[None, None], scores, -1000000000.0)
            p = _softmax(scores, axis=-1)
            o = torch.einsum('bhqk,bhke->bhqe', p, v)
            o = torch.einsum('bhte,hed->btd', o, Wo)
            mem = o[:, :M, :].clone()
            outs.append(o[:, M:, :])
        return torch.concatenate(outs, axis=1)

    def _B_ref(x, wqkv, wo):
        B, S, D = x.shape
        H = 32
        Dh = D // H
        W = 256
        qkv = (lambda x, *d: x.permute(*d))((x @ wqkv).reshape(B, S, 3, H, Dh), 2, 0, 3, 1, 4)
        q, k, v = (qkv[0], qkv[1], qkv[2])
        scores = torch.einsum('bhqd,bhkd->bhqk', q, k) * Dh ** (-0.5)
        idx = torch.arange(S)
        mask = (idx[:, None] - idx[None, :] >= 0) & (idx[:, None] - idx[None, :] < W)
        scores = torch.where(mask[None, None], scores, -1000000000.0)
        p = _softmax(scores, axis=-1)
        o = torch.einsum('bhqk,bhkd->bhqd', p, v)
        return (lambda x, *d: x.permute(*d))(o, 0, 2, 1, 3).reshape(B, S, D) @ wo
    x = A_x
    g = _sigmoid((x * gate_w).sum(-1, keepdims=True))
    a = _A_ref(x, A_mem0, A_Wq, A_Wk, A_Wv, A_Wo, A_seg_len)
    b = _B_ref(x, B_wqkv, B_wo)
    return g * a + (1 - g) * b
```

```cpp
#include <hip/hip_runtime.h>
#include <hip/hip_bf16.h>
#include <stdint.h>

typedef unsigned v4u __attribute__((ext_vector_type(4)));
typedef unsigned v2u __attribute__((ext_vector_type(2)));
typedef float v4f __attribute__((ext_vector_type(4)));
typedef float v2f __attribute__((ext_vector_type(2)));
typedef float v16f __attribute__((ext_vector_type(16)));
typedef __bf16 bf8 __attribute__((ext_vector_type(8)));
typedef __bf16 bf2 __attribute__((ext_vector_type(2)));
typedef short s4 __attribute__((ext_vector_type(4)));
#define LAS __attribute__((address_space(3)))
#define GAS __attribute__((address_space(1)))
typedef __amdgpu_buffer_rsrc_t rsrc_t;

constexpr unsigned WS_E = 0u;
constexpr unsigned WS_RDY = 1024u;
constexpr unsigned WS_PPROG = 4096u;
constexpr unsigned WS_BPROG = 28672u;
constexpr unsigned WS_GRDY = 36864u;
constexpr unsigned WS_TPROG = 45056u;
constexpr unsigned WS_ABORT = 61440u;
constexpr unsigned WS_HPROG = 65536u;
constexpr unsigned WS_GBUF = 131072u;
constexpr unsigned WS_BCST = 262144u;
constexpr unsigned WS_INBOX = 2097152u;
constexpr unsigned WS_MEMBOX = 3145728u;
constexpr unsigned WS_OX = 4194304u;
constexpr unsigned WS_OB = 37748736u;
constexpr unsigned WS_KM = 39845888u;
constexpr unsigned WS_VM = 44040192u;
constexpr unsigned WS_QB = 52428800u;
constexpr unsigned WS_KB = 54525952u;
constexpr unsigned WS_VB = 56623104u;
constexpr unsigned WS_QX = 58720256u;
constexpr unsigned WS_KX = 75497472u;
constexpr unsigned WS_VX = 92274688u;
constexpr unsigned WS_TRACE = 109051904u;
constexpr unsigned WS_END = 113246208u;
constexpr unsigned SPIN = 400000, SENT = 0x7F817F81u;
constexpr int LDS_BYTES = 160 * 1024;

struct Params {
    const void* x; const void* mem0; const void* wq; const void* wk; const void* wv; const void* wo;
    const void* bqkv; const void* bwo; const void* gate; void* out; unsigned char* ws;
};

__device__ __forceinline__ unsigned pk(float a, float b) { return __builtin_bit_cast(unsigned, __builtin_convertvector((v2f){a, b}, bf2)); }
__device__ __forceinline__ bf8 asbf8(v4u u) { return __builtin_bit_cast(bf8, u); }
__device__ __forceinline__ v4f mfma32(v4u a, v4u b, v4f c) { return __builtin_amdgcn_mfma_f32_16x16x32_bf16(asbf8(a), asbf8(b), c, 0, 0, 0); }
__device__ __forceinline__ v4f mfma16(v2u a, v2u b, v4f c) { return mfma32((v4u){a.x, a.y, 0u, 0u}, (v4u){b.x, b.y, 0u, 0u}, c); }
__device__ __forceinline__ v16f mfmaW(v4u a, v4u b, v16f c) { return __builtin_amdgcn_mfma_f32_32x32x16_bf16(asbf8(a), asbf8(b), c, 0, 0, 0); }
__device__ __forceinline__ v2u trread(LAS unsigned char* p) { return __builtin_bit_cast(v2u, __builtin_amdgcn_ds_read_tr16_b64_v4i16((LAS s4*)p)); }
__device__ __forceinline__ v4u ldg16(rsrc_t r, unsigned off) { return __builtin_amdgcn_raw_buffer_load_b128(r, off, 0, 16); }
__device__ __forceinline__ v2u ldg8(rsrc_t r, unsigned off) { return __builtin_amdgcn_raw_buffer_load_b64(r, off, 0, 16); }
__device__ __forceinline__ unsigned ldg4(rsrc_t r, unsigned off) { return __builtin_amdgcn_raw_buffer_load_b32(r, off, 0, 16); }
__device__ __forceinline__ void stg16(v4u v, rsrc_t r, unsigned off) { __builtin_amdgcn_raw_buffer_store_b128(v, r, off, 0, 16); }
__device__ __forceinline__ void stg8(v2u v, rsrc_t r, unsigned off) { __builtin_amdgcn_raw_buffer_store_b64(v, r, off, 0, 16); }
__device__ __forceinline__ void stg4(unsigned v, rsrc_t r, unsigned off) { __builtin_amdgcn_raw_buffer_store_b32(v, r, off, 0, 16); }
__device__ __forceinline__ float fb(unsigned u) { return __builtin_bit_cast(float, u); }
__device__ __forceinline__ unsigned ub(float f) { return __builtin_bit_cast(unsigned, f); }
#define TR(slot, val) do {} while (0)
#define RT() 0
#define SEG(i) do {} while (0)
#define CKV() 0
#define SEGDECL do {} while (0)
#define SEGOUT(base) do {} while (0)
#define DRAIN() asm volatile("s_waitcnt vmcnt(0)" ::: "memory")
#define BARRIER() asm volatile("s_waitcnt lgkmcnt(0)\n\ts_barrier" ::: "memory")
__device__ __forceinline__ unsigned rfl(unsigned v) { return __builtin_amdgcn_readfirstlane(v); }
__device__ __forceinline__ float x32max(float v) { auto r = __builtin_amdgcn_permlane32_swap(ub(v), ub(v), false, false); return fmaxf(fb(r[0]), fb(r[1])); }
__device__ __forceinline__ float x16max(float v) { auto r = __builtin_amdgcn_permlane16_swap(ub(v), ub(v), false, false); return fmaxf(fb(r[0]), fb(r[1])); }
__device__ __forceinline__ float x32add(float v) { auto r = __builtin_amdgcn_permlane32_swap(ub(v), ub(v), false, false); return fb(r[0]) + fb(r[1]); }
__device__ __forceinline__ float x16add(float v) { auto r = __builtin_amdgcn_permlane16_swap(ub(v), ub(v), false, false); return fb(r[0]) + fb(r[1]); }

__device__ __forceinline__ void dma16(unsigned ldsaddr, unsigned voff, v4u srd, unsigned soff) {
    asm volatile("s_mov_b32 m0, %0\n\ts_nop 0\n\tbuffer_load_dwordx4 %1, %2, %3 offen lds"
                 :: "s"(ldsaddr), "v"(voff), "s"(srd), "s"(soff) : "memory");
}
__device__ __forceinline__ v4u mksrd(const void* p, unsigned bytes) {
    const uint64_t a = (uint64_t)p;
    return (v4u){rfl((unsigned)a), rfl((unsigned)(a >> 32) & 0xffffu), bytes, 0x00020000u};
}
__device__ __forceinline__ unsigned wait_flag(rsrc_t ws, unsigned off, unsigned tag, unsigned need, unsigned code) {
    for (unsigned sp = 0; sp < SPIN; ++sp) {
        asm volatile("" ::: "memory");
        const v2u f = ldg8(ws, off);
        const unsigned t = rfl(f.y), v = rfl(f.x);
        if (t == tag && v >= need) { asm volatile("" ::: "memory"); return v; }
        __builtin_amdgcn_s_sleep(2);
    }
    stg4(code, ws, WS_ABORT);
    return 0;
}
__device__ __forceinline__ unsigned wait_flag2(rsrc_t ws, unsigned off, unsigned tag, unsigned need, unsigned code) {
    for (unsigned sp = 0; sp < SPIN; ++sp) {
        asm volatile("" ::: "memory");
        const v2u f = ldg8(ws, off);
        const unsigned x = f.y == tag ? f.x : 0u;
        const unsigned v = min((unsigned)__builtin_amdgcn_readlane((int)x, 0), (unsigned)__builtin_amdgcn_readlane((int)x, 1));
        if (v >= need) { asm volatile("" ::: "memory"); return v; }
        __builtin_amdgcn_s_sleep(1);
    }
    stg4(code, ws, WS_ABORT);
    return 0;
}
__device__ __forceinline__ void set_flag(rsrc_t ws, unsigned off, unsigned tag, unsigned val) { stg8((v2u){val, tag}, ws, off); }

__device__ __forceinline__ void role_head(const Params& p, int h, int c2, int w) {
    const uint64_t ws = (uint64_t)p.ws, wq = (uint64_t)p.wq, wk = (uint64_t)p.wk, wv = (uint64_t)p.wv, wo = (uint64_t)p.wo;
    asm volatile(
        "s_mov_b32 s40, %[wslo]\n"
        "s_and_b32 s41, %[wshi], 0xffff\n"
        "s_mov_b32 s42, 113246208\n"
        "s_mov_b32 s43, 0x20000\n"
        "s_mov_b32 s44, %[h]\n"
        "s_mov_b32 s45, %[w]\n"
        "s_mov_b32 s79, %[c2]\n"
        "s_mov_b32 s99, 0\n"
        "v_mbcnt_lo_u32_b32 v104, -1, 0\n"
        "v_mbcnt_hi_u32_b32 v104, -1, v104\n"
        "s_and_b32 s58, s45, 1\n"
        "s_lshr_b32 s59, s45, 1\n"
        "v_lshrrev_b32 v107, 4, v104\n"
        "v_lshlrev_b32 v108, 1, v107\n"
        "s_lshl_b32 s73, s58, 3\n"
        "v_or_b32 v108, s73, v108\n"
        "v_and_b32 v109, 15, v104\n"
        "v_xor_b32 v108, v108, v109\n"
        "v_lshlrev_b32 v108, 4, v108\n"
        "v_and_b32 v109, 1, v107\n"
        "v_lshrrev_b32 v110, 5, v104\n"
        "s_lshl_b32 s72, s59, 1\n"
        "s_lshl_b32 s73, s58, 2\n"
        "s_add_u32 s73, s73, s72\n"
        "v_lshl_add_u32 v111, v110, 4, v109\n"
        "v_add_u32 v111, s73, v111\n"
        "v_lshl_add_u32 v105, v111, 13, v108\n"
        "v_lshl_add_u32 v106, v111, 10, v108\n"
        "s_mov_b32 s70, 4194304\n"
        "s_mov_b32 s71, 0x20000\n"
        "s_lshl_b32 s52, s44, 8\n"
        "s_lshl_b32 s54, s45, 13\n"
        "s_add_u32 s54, s54, 0\n"
        "s_cmp_lg_u32 s79, 0\n"
        "s_cbranch_scc1 LH_F1\n"
        "s_mov_b32 s68, %[wqlo]\n"
        "s_and_b32 s69, %[wqhi], 0xffff\n"
        "s_add_u32 m0, s54, 32768\n"
        "s_add_u32 s53, s52, 0\n"
        "buffer_load_dwordx4 v105, s[68:71], s53 offen lds\n"
        "s_add_u32 m0, s54, 33792\n"
        "s_add_u32 s53, s52, 65536\n"
        "buffer_load_dwordx4 v105, s[68:71], s53 offen lds\n"
        "s_add_u32 m0, s54, 34816\n"
        "s_add_u32 s53, s52, 262144\n"
        "buffer_load_dwordx4 v105, s[68:71], s53 offen lds\n"
        "s_add_u32 m0, s54, 35840\n"
        "s_add_u32 s53, s52, 327680\n"
        "buffer_load_dwordx4 v105, s[68:71], s53 offen lds\n"
        "s_add_u32 m0, s54, 36864\n"
        "s_add_u32 s53, s52, 524288\n"
        "buffer_load_dwordx4 v105, s[68:71], s53 offen lds\n"
        "s_add_u32 m0, s54, 37888\n"
        "s_add_u32 s53, s52, 589824\n"
        "buffer_load_dwordx4 v105, s[68:71], s53 offen lds\n"
        "s_add_u32 m0, s54, 38912\n"
        "s_add_u32 s53, s52, 786432\n"
        "buffer_load_dwordx4 v105, s[68:71], s53 offen lds\n"
        "s_add_u32 m0, s54, 39936\n"
        "s_add_u32 s53, s52, 851968\n"
        "buffer_load_dwordx4 v105, s[68:71], s53 offen lds\n"
        "s_add_u32 m0, s54, 65536\n"
        "s_add_u32 s53, s52, 1048576\n"
        "buffer_load_dwordx4 v105, s[68:71], s53 offen lds\n"
        "s_add_u32 m0, s54, 66560\n"
        "s_add_u32 s53, s52, 1114112\n"
        "buffer_load_dwordx4 v105, s[68:71], s53 offen lds\n"
        "s_add_u32 m0, s54, 67584\n"
        "s_add_u32 s53, s52, 1310720\n"
        "buffer_load_dwordx4 v105, s[68:71], s53 offen lds\n"
        "s_add_u32 m0, s54, 68608\n"
        "s_add_u32 s53, s52, 1376256\n"
        "buffer_load_dwordx4 v105, s[68:71], s53 offen lds\n"
        "s_add_u32 m0, s54, 69632\n"
        "s_add_u32 s53, s52, 1572864\n"
        "buffer_load_dwordx4 v105, s[68:71], s53 offen lds\n"
        "s_add_u32 m0, s54, 70656\n"
        "s_add_u32 s53, s52, 1638400\n"
        "buffer_load_dwordx4 v105, s[68:71], s53 offen lds\n"
        "s_add_u32 m0, s54, 71680\n"
        "s_add_u32 s53, s52, 1835008\n"
        "buffer_load_dwordx4 v105, s[68:71], s53 offen lds\n"
        "s_add_u32 m0, s54, 72704\n"
        "s_add_u32 s53, s52, 1900544\n"
        "buffer_load_dwordx4 v105, s[68:71], s53 offen lds\n"
        "s_add_u32 m0, s54, 98304\n"
        "s_add_u32 s53, s52, 2097152\n"
        "buffer_load_dwordx4 v105, s[68:71], s53 offen lds\n"
        "s_add_u32 m0, s54, 99328\n"
        "s_add_u32 s53, s52, 2162688\n"
        "buffer_load_dwordx4 v105, s[68:71], s53 offen lds\n"
        "s_add_u32 m0, s54, 100352\n"
        "s_add_u32 s53, s52, 2359296\n"
        "buffer_load_dwordx4 v105, s[68:71], s53 offen lds\n"
        "s_add_u32 m0, s54, 101376\n"
        "s_add_u32 s53, s52, 2424832\n"
        "buffer_load_dwordx4 v105, s[68:71], s53 offen lds\n"
        "s_add_u32 m0, s54, 102400\n"
        "s_add_u32 s53, s52, 2621440\n"
        "buffer_load_dwordx4 v105, s[68:71], s53 offen lds\n"
        "s_add_u32 m0, s54, 103424\n"
        "s_add_u32 s53, s52, 2686976\n"
        "buffer_load_dwordx4 v105, s[68:71], s53 offen lds\n"
        "s_add_u32 m0, s54, 104448\n"
        "s_add_u32 s53, s52, 2883584\n"
        "buffer_load_dwordx4 v105, s[68:71], s53 offen lds\n"
        "s_add_u32 m0, s54, 105472\n"
        "s_add_u32 s53, s52, 2949120\n"
        "buffer_load_dwordx4 v105, s[68:71], s53 offen lds\n"
        "s_add_u32 m0, s54, 131072\n"
        "s_add_u32 s53, s52, 3145728\n"
        "buffer_load_dwordx4 v105, s[68:71], s53 offen lds\n"
        "s_add_u32 m0, s54, 132096\n"
        "s_add_u32 s53, s52, 3211264\n"
        "buffer_load_dwordx4 v105, s[68:71], s53 offen lds\n"
        "s_add_u32 m0, s54, 133120\n"
        "s_add_u32 s53, s52, 3407872\n"
        "buffer_load_dwordx4 v105, s[68:71], s53 offen lds\n"
        "s_add_u32 m0, s54, 134144\n"
        "s_add_u32 s53, s52, 3473408\n"
        "buffer_load_dwordx4 v105, s[68:71], s53 offen lds\n"
        "s_add_u32 m0, s54, 135168\n"
        "s_add_u32 s53, s52, 3670016\n"
        "buffer_load_dwordx4 v105, s[68:71], s53 offen lds\n"
        "s_add_u32 m0, s54, 136192\n"
        "s_add_u32 s53, s52, 3735552\n"
        "buffer_load_dwordx4 v105, s[68:71], s53 offen lds\n"
        "s_add_u32 m0, s54, 137216\n"
        "s_add_u32 s53, s52, 3932160\n"
        "buffer_load_dwordx4 v105, s[68:71], s53 offen lds\n"
        "s_add_u32 m0, s54, 138240\n"
        "s_add_u32 s53, s52, 3997696\n"
        "buffer_load_dwordx4 v105, s[68:71], s53 offen lds\n"
        "s_mov_b32 s68, %[wklo]\n"
        "s_and_b32 s69, %[wkhi], 0xffff\n"
        "s_add_u32 m0, s54, 0\n"
        "s_add_u32 s53, s52, 0\n"
        "buffer_load_dwordx4 v105, s[68:71], s53 offen lds\n"
        "s_add_u32 m0, s54, 1024\n"
        "s_add_u32 s53, s52, 65536\n"
        "buffer_load_dwordx4 v105, s[68:71], s53 offen lds\n"
        "s_add_u32 m0, s54, 2048\n"
        "s_add_u32 s53, s52, 262144\n"
        "buffer_load_dwordx4 v105, s[68:71], s53 offen lds\n"
        "s_add_u32 m0, s54, 3072\n"
        "s_add_u32 s53, s52, 327680\n"
        "buffer_load_dwordx4 v105, s[68:71], s53 offen lds\n"
        "s_add_u32 m0, s54, 4096\n"
        "s_add_u32 s53, s52, 524288\n"
        "buffer_load_dwordx4 v105, s[68:71], s53 offen lds\n"
        "s_add_u32 m0, s54, 5120\n"
        "s_add_u32 s53, s52, 589824\n"
        "buffer_load_dwordx4 v105, s[68:71], s53 offen lds\n"
        "s_add_u32 m0, s54, 6144\n"
        "s_add_u32 s53, s52, 786432\n"
        "buffer_load_dwordx4 v105, s[68:71], s53 offen lds\n"
        "s_add_u32 m0, s54, 7168\n"
        "s_add_u32 s53, s52, 851968\n"
        "buffer_load_dwordx4 v105, s[68:71], s53 offen lds\n"
        "s_branch LH_F2\n"
        "LH_F1:\n"
        "s_mov_b32 s68, %[wklo]\n"
        "s_and_b32 s69, %[wkhi], 0xffff\n"
        "s_add_u32 m0, s54, 32768\n"
        "s_add_u32 s53, s52, 2097152\n"
        "buffer_load_dwordx4 v105, s[68:71], s53 offen lds\n"
        "s_add_u32 m0, s54, 33792\n"
        "s_add_u32 s53, s52, 2162688\n"
        "buffer_load_dwordx4 v105, s[68:71], s53 offen lds\n"
        "s_add_u32 m0, s54, 34816\n"
        "s_add_u32 s53, s52, 2359296\n"
        "buffer_load_dwordx4 v105, s[68:71], s53 offen lds\n"
        "s_add_u32 m0, s54, 35840\n"
        "s_add_u32 s53, s52, 2424832\n"
        "buffer_load_dwordx4 v105, s[68:71], s53 offen lds\n"
        "s_add_u32 m0, s54, 36864\n"
        "s_add_u32 s53, s52, 2621440\n"
        "buffer_load_dwordx4 v105, s[68:71], s53 offen lds\n"
        "s_add_u32 m0, s54, 37888\n"
        "s_add_u32 s53, s52, 2686976\n"
        "buffer_load_dwordx4 v105, s[68:71], s53 offen lds\n"
        "s_add_u32 m0, s54, 38912\n"
        "s_add_u32 s53, s52, 2883584\n"
        "buffer_load_dwordx4 v105, s[68:71], s53 offen lds\n"
        "s_add_u32 m0, s54, 39936\n"
        "s_add_u32 s53, s52, 2949120\n"
        "buffer_load_dwordx4 v105, s[68:71], s53 offen lds\n"
        "s_add_u32 m0, s54, 65536\n"
        "s_add_u32 s53, s52, 3145728\n"
        "buffer_load_dwordx4 v105, s[68:71], s53 offen lds\n"
        "s_add_u32 m0, s54, 66560\n"
        "s_add_u32 s53, s52, 3211264\n"
        "buffer_load_dwordx4 v105, s[68:71], s53 offen lds\n"
        "s_add_u32 m0, s54, 67584\n"
        "s_add_u32 s53, s52, 3407872\n"
        "buffer_load_dwordx4 v105, s[68:71], s53 offen lds\n"
        "s_add_u32 m0, s54, 68608\n"
        "s_add_u32 s53, s52, 3473408\n"
        "buffer_load_dwordx4 v105, s[68:71], s53 offen lds\n"
        "s_add_u32 m0, s54, 69632\n"
        "s_add_u32 s53, s52, 3670016\n"
        "buffer_load_dwordx4 v105, s[68:71], s53 offen lds\n"
        "s_add_u32 m0, s54, 70656\n"
        "s_add_u32 s53, s52, 3735552\n"
        "buffer_load_dwordx4 v105, s[68:71], s53 offen lds\n"
        "s_add_u32 m0, s54, 71680\n"
        "s_add_u32 s53, s52, 3932160\n"
        "buffer_load_dwordx4 v105, s[68:71], s53 offen lds\n"
        "s_add_u32 m0, s54, 72704\n"
        "s_add_u32 s53, s52, 3997696\n"
        "buffer_load_dwordx4 v105, s[68:71], s53 offen lds\n"
        "s_mov_b32 s68, %[wvlo]\n"
        "s_and_b32 s69, %[wvhi], 0xffff\n"
        "s_add_u32 m0, s54, 98304\n"
        "s_add_u32 s53, s52, 0\n"
        "buffer_load_dwordx4 v105, s[68:71], s53 offen lds\n"
        "s_add_u32 m0, s54, 99328\n"
        "s_add_u32 s53, s52, 65536\n"
        "buffer_load_dwordx4 v105, s[68:71], s53 offen lds\n"
        "s_add_u32 m0, s54, 100352\n"
        "s_add_u32 s53, s52, 262144\n"
        "buffer_load_dwordx4 v105, s[68:71], s53 offen lds\n"
        "s_add_u32 m0, s54, 101376\n"
        "s_add_u32 s53, s52, 327680\n"
        "buffer_load_dwordx4 v105, s[68:71], s53 offen lds\n"
        "s_add_u32 m0, s54, 102400\n"
        "s_add_u32 s53, s52, 524288\n"
        "buffer_load_dwordx4 v105, s[68:71], s53 offen lds\n"
        "s_add_u32 m0, s54, 103424\n"
        "s_add_u32 s53, s52, 589824\n"
        "buffer_load_dwordx4 v105, s[68:71], s53 offen lds\n"
        "s_add_u32 m0, s54, 104448\n"
        "s_add_u32 s53, s52, 786432\n"
        "buffer_load_dwordx4 v105, s[68:71], s53 offen lds\n"
        "s_add_u32 m0, s54, 105472\n"
        "s_add_u32 s53, s52, 851968\n"
        "buffer_load_dwordx4 v105, s[68:71], s53 offen lds\n"
        "s_add_u32 m0, s54, 131072\n"
        "s_add_u32 s53, s52, 1048576\n"
        "buffer_load_dwordx4 v105, s[68:71], s53 offen lds\n"
        "s_add_u32 m0, s54, 132096\n"
        "s_add_u32 s53, s52, 1114112\n"
        "buffer_load_dwordx4 v105, s[68:71], s53 offen lds\n"
        "s_add_u32 m0, s54, 133120\n"
        "s_add_u32 s53, s52, 1310720\n"
        "buffer_load_dwordx4 v105, s[68:71], s53 offen lds\n"
        "s_add_u32 m0, s54, 134144\n"
        "s_add_u32 s53, s52, 1376256\n"
        "buffer_load_dwordx4 v105, s[68:71], s53 offen lds\n"
        "s_add_u32 m0, s54, 135168\n"
        "s_add_u32 s53, s52, 1572864\n"
        "buffer_load_dwordx4 v105, s[68:71], s53 offen lds\n"
        "s_add_u32 m0, s54, 136192\n"
        "s_add_u32 s53, s52, 1638400\n"
        "buffer_load_dwordx4 v105, s[68:71], s53 offen lds\n"
        "s_add_u32 m0, s54, 137216\n"
        "s_add_u32 s53, s52, 1835008\n"
        "buffer_load_dwordx4 v105, s[68:71], s53 offen lds\n"
        "s_add_u32 m0, s54, 138240\n"
        "s_add_u32 s53, s52, 1900544\n"
        "buffer_load_dwordx4 v105, s[68:71], s53 offen lds\n"
        "s_add_u32 m0, s54, 0\n"
        "s_add_u32 s53, s52, 2097152\n"
        "buffer_load_dwordx4 v105, s[68:71], s53 offen lds\n"
        "s_add_u32 m0, s54, 1024\n"
        "s_add_u32 s53, s52, 2162688\n"
        "buffer_load_dwordx4 v105, s[68:71], s53 offen lds\n"
        "s_add_u32 m0, s54, 2048\n"
        "s_add_u32 s53, s52, 2359296\n"
        "buffer_load_dwordx4 v105, s[68:71], s53 offen lds\n"
        "s_add_u32 m0, s54, 3072\n"
        "s_add_u32 s53, s52, 2424832\n"
        "buffer_load_dwordx4 v105, s[68:71], s53 offen lds\n"
        "s_add_u32 m0, s54, 4096\n"
        "s_add_u32 s53, s52, 2621440\n"
        "buffer_load_dwordx4 v105, s[68:71], s53 offen lds\n"
        "s_add_u32 m0, s54, 5120\n"
        "s_add_u32 s53, s52, 2686976\n"
        "buffer_load_dwordx4 v105, s[68:71], s53 offen lds\n"
        "s_add_u32 m0, s54, 6144\n"
        "s_add_u32 s53, s52, 2883584\n"
        "buffer_load_dwordx4 v105, s[68:71], s53 offen lds\n"
        "s_add_u32 m0, s54, 7168\n"
        "s_add_u32 s53, s52, 2949120\n"
        "buffer_load_dwordx4 v105, s[68:71], s53 offen lds\n"
        "LH_F2:\n"
        "v_lshrrev_b32 v107, 4, v104\n"
        "v_and_b32 v108, 15, v104\n"
        "v_lshlrev_b32 v0, 4, v104\n"
        "v_lshlrev_b32 v10, 3, v104\n"
        "v_lshlrev_b32 v5, 2, v104\n"
        "s_lshl_b32 s58, s45, 10\n"
        "v_add_u32 v1, s58, v0\n"
        "v_add_u32 v10, s58, v10\n"
        "v_lshrrev_b32 v6, 2, v1\n"
        "s_add_u32 s58, s54, 98304\n"
        "v_add_u32 v2, s58, v0\n"
        "s_mov_b32 s62, 0xaaaaaaaa\n"
        "s_mov_b32 s63, 0xaaaaaaaa\n"
        "s_mov_b32 s66, -1\n"
        "s_mov_b32 s67, 0\n"
        "s_mov_b32 s60, 0x3f803f80\n"
        "s_mov_b32 s61, 0x3f803f80\n"
        "v_mov_b32 v3, 0x4444\n"
        "v_mov_b32 v109, 0xeeee\n"
        "v_cndmask_b32 v3, v3, v109, s[62:63]\n"
        "v_lshrrev_b32 v114, 2, v104\n"
        "v_and_b32 v114, 3, v114\n"
        "v_and_b32 v115, 1, v107\n"
        "v_lshrrev_b32 v116, 5, v104\n"
        "v_lshl_add_u32 v110, v116, 2, v114\n"
        "v_lshl_add_u32 v110, v115, 5, v110\n"
        "v_lshlrev_b32 v110, 8, v110\n"
        "v_lshl_add_u32 v116, v115, 2, v114\n"
        "v_lshlrev_b32 v116, 1, v116\n"
        "v_lshrrev_b32 v117, 1, v104\n"
        "v_and_b32 v117, 1, v117\n"
        "s_lshl_b32 s58, s45, 2\n"
        "v_or_b32 v117, s58, v117\n"
        "v_xor_b32 v116, v116, v117\n"
        "v_lshl_add_u32 v110, v116, 4, v110\n"
        "v_and_b32 v116, 1, v104\n"
        "v_lshl_add_u32 v110, v116, 3, v110\n"
        "v_add_u32 v110, 0, v110\n"
        "v_xor_b32 v111, 32, v110\n"
        "v_add_u32 v112, 0x10000, v110\n"
        "v_add_u32 v113, 0x10000, v111\n"
        "v_lshrrev_b32 v117, 1, v108\n"
        "v_lshlrev_b32 v11, 7, v107\n"
        "v_lshl_add_u32 v11, v117, 4, v11\n"
        "v_mov_b32 v118, 2146435072\n"
        "v_cndmask_b32 v11, v118, v11, s[62:63]\n"
        "v_lshlrev_b32 v7, 5, v108\n"
        "v_lshrrev_b32 v118, 3, v108\n"
        "v_lshlrev_b32 v118, 7, v118\n"
        "v_xor_b32 v7, v7, v118\n"
        "v_lshl_add_u32 v7, v107, 3, v7\n"
        "s_lshl_b32 s58, s45, 9\n"
        "s_add_u32 s58, s58, 14336\n"
        "v_add_u32 v7, s58, v7\n"
        "v_lshl_add_u32 v8, v107, 3, v114\n"
        "v_lshlrev_b32 v8, 5, v8\n"
        "v_lshlrev_b32 v118, 7, v115\n"
        "v_xor_b32 v8, v8, v118\n"
        "v_and_b32 v118, 3, v104\n"
        "v_lshl_add_u32 v8, v118, 3, v8\n"
        "v_add_u32 v8, 14336, v8\n"
        "v_xor_b32 v9, 0x80, v8\n"
        "v_lshrrev_b32 v118, 5, v104\n"
        "v_lshlrev_b32 v13, 10, v118\n"
        "v_lshrrev_b32 v118, 2, v108\n"
        "v_lshl_add_u32 v13, v118, 8, v13\n"
        "v_and_b32 v118, 1, v118\n"
        "v_lshl_add_u32 v13, v118, 6, v13\n"
        "v_lshl_add_u32 v13, v115, 7, v13\n"
        "v_lshl_add_u32 v13, v116, 5, v13\n"
        "v_and_b32 v118, 1, v117\n"
        "v_lshl_add_u32 v13, v118, 4, v13\n"
        "v_xor_b32 v14, 64, v13\n"
        "s_lshl_b32 s78, s45, 10\n"
        "v_lshrrev_b32 v118, 2, v117\n"
        "v_lshlrev_b32 v118, 1, v118\n"
        "v_and_b32 v119, 2, v117\n"
        "v_lshl_add_u32 v118, v119, 1, v118\n"
        "v_and_b32 v119, 1, v117\n"
        "v_add_u32 v118, v118, v119\n"
        "v_cndmask_b32 v118, v117, v118, s[62:63]\n"
        "v_lshrrev_b32 v119, 1, v118\n"
        "v_and_b32 v118, 1, v118\n"
        "v_lshlrev_b32 v118, 4, v118\n"
        "v_mov_b32 v120, 0x3f80\n"
        "v_lshlrev_b32 v118, v118, v120\n"
        "v_cmp_eq_u32 vcc, v119, v107\n"
        "s_nop 1\n"
        "v_cndmask_b32 v118, 0, v118, vcc\n"
        "v_cndmask_b32 v119, v118, 0, s[62:63]\n"
        "v_cndmask_b32 v118, 0, v118, s[62:63]\n"
        "v_mov_b32 v92, v119\n"
        "v_mov_b32 v93, 0\n"
        "v_mov_b32 v94, v118\n"
        "v_mov_b32 v95, 0\n"
        "v_mov_b32 v96, 0\n"
        "v_mov_b32 v97, v119\n"
        "v_mov_b32 v98, 0\n"
        "v_mov_b32 v99, v118\n"
        "s_mov_b32 s72, 0x3e0293ee\n"
        "s_xor_b32 s76, s72, 0x80000000\n"
        "v_lshl_add_u32 v118, v107, 1, v116\n"
        "v_lshlrev_b32 v118, 10, v118\n"
        "v_lshrrev_b32 v119, 2, v104\n"
        "v_xor_b32 v119, v119, v107\n"
        "v_and_b32 v119, 1, v119\n"
        "v_lshl_add_u32 v118, v119, 8, v118\n"
        "v_and_b32 v119, 1, v117\n"
        "v_lshl_add_u32 v118, v119, 5, v118\n"
        "v_lshrrev_b32 v119, 3, v108\n"
        "v_lshl_add_u32 v118, v119, 4, v118\n"
        "s_lshl_b32 s58, s45, 6\n"
        "v_add_u32 v118, s58, v118\n"
        "s_mov_b64 s[64:65], %[mem0]\n"
        "global_load_dwordx4 v[120:123], v118, s[64:65]\n"
        "global_load_dwordx4 v[124:127], v118, s[64:65] offset:512\n"
        "v_mov_b32 v118, 0\n"
        "buffer_load_dword v19, v118, s[40:43], 0 offen sc1\n"
        "v_mov_b32 v119, v1\n"
        "s_lshl_b32 s58, s44, 17\n"
        "s_add_u32 s58, s58, 39845888\n"
        "buffer_load_dword v100, v5, s[40:43], s58 offen sc1\n"
        "s_lshl_b32 s58, s44, 18\n"
        "s_add_u32 s58, s58, 44040192\n"
        "buffer_load_dword v100, v5, s[40:43], s58 offen sc1\n"
        "s_mov_b32 s58, 3244032\n"
        "buffer_load_dword v100, v5, s[40:43], s58 offen sc1\n"
        "s_lshl_b32 s58, s44, 7\n"
        "v_and_b32 v12, 1, v104\n"
        "v_lshlrev_b32 v12, 12, v12\n"
        "v_add_u32 v12, s58, v12\n"
        "s_mov_b32 s77, 8192\n"
        "v_add_u32 v102, 0x20000, v110\n"
        "v_add_u32 v103, 0x20000, v111\n"
        "s_cmp_lg_u32 s79, 0\n"
        "s_cbranch_scc1 LH_G1\n"
        "s_waitcnt vmcnt(38)\n"
        "s_barrier\n"
        "ds_read_b64_tr_b16 a[0:1], v110 offset:32768\n"
        "ds_read_b64_tr_b16 a[2:3], v110 offset:49152\n"
        "ds_read_b64_tr_b16 a[4:5], v110 offset:34816\n"
        "ds_read_b64_tr_b16 a[6:7], v110 offset:51200\n"
        "ds_read_b64_tr_b16 a[64:65], v111 offset:32768\n"
        "ds_read_b64_tr_b16 a[66:67], v111 offset:49152\n"
        "ds_read_b64_tr_b16 a[68:69], v111 offset:34816\n"
        "ds_read_b64_tr_b16 a[70:71], v111 offset:51200\n"
        "ds_read_b64_tr_b16 a[8:9], v110 offset:36864\n"
        "ds_read_b64_tr_b16 a[10:11], v110 offset:53248\n"
        "ds_read_b64_tr_b16 a[12:13], v110 offset:38912\n"
        "ds_read_b64_tr_b16 a[14:15], v110 offset:55296\n"
        "ds_read_b64_tr_b16 a[72:73], v111 offset:36864\n"
        "ds_read_b64_tr_b16 a[74:75], v111 offset:53248\n"
        "ds_read_b64_tr_b16 a[76:77], v111 offset:38912\n"
        "ds_read_b64_tr_b16 a[78:79], v111 offset:55296\n"
        "s_waitcnt lgkmcnt(0)\n"
        "s_barrier\n"
        "s_add_u32 m0, s54, 32768\n"
        "s_add_u32 s53, s52, 1048576\n"
        "buffer_load_dwordx4 v105, s[68:71], s53 offen lds\n"
        "s_add_u32 m0, s54, 33792\n"
        "s_add_u32 s53, s52, 1114112\n"
        "buffer_load_dwordx4 v105, s[68:71], s53 offen lds\n"
        "s_add_u32 m0, s54, 34816\n"
        "s_add_u32 s53, s52, 1310720\n"
        "buffer_load_dwordx4 v105, s[68:71], s53 offen lds\n"
        "s_add_u32 m0, s54, 35840\n"
        "s_add_u32 s53, s52, 1376256\n"
        "buffer_load_dwordx4 v105, s[68:71], s53 offen lds\n"
        "s_add_u32 m0, s54, 36864\n"
        "s_add_u32 s53, s52, 1572864\n"
        "buffer_load_dwordx4 v105, s[68:71], s53 offen lds\n"
        "s_add_u32 m0, s54, 37888\n"
        "s_add_u32 s53, s52, 1638400\n"
        "buffer_load_dwordx4 v105, s[68:71], s53 offen lds\n"
        "s_add_u32 m0, s54, 38912\n"
        "s_add_u32 s53, s52, 1835008\n"
        "buffer_load_dwordx4 v105, s[68:71], s53 offen lds\n"
        "s_add_u32 m0, s54, 39936\n"
        "s_add_u32 s53, s52, 1900544\n"
        "buffer_load_dwordx4 v105, s[68:71], s53 offen lds\n"
        "s_waitcnt vmcnt(38)\n"
        "s_barrier\n"
        "ds_read_b64_tr_b16 a[16:17], v112 offset:0\n"
        "ds_read_b64_tr_b16 a[18:19], v112 offset:16384\n"
        "ds_read_b64_tr_b16 a[20:21], v112 offset:2048\n"
        "ds_read_b64_tr_b16 a[22:23], v112 offset:18432\n"
        "ds_read_b64_tr_b16 a[80:81], v113 offset:0\n"
        "ds_read_b64_tr_b16 a[82:83], v113 offset:16384\n"
        "ds_read_b64_tr_b16 a[84:85], v113 offset:2048\n"
        "ds_read_b64_tr_b16 a[86:87], v113 offset:18432\n"
        "ds_read_b64_tr_b16 a[24:25], v112 offset:4096\n"
        "ds_read_b64_tr_b16 a[26:27], v112 offset:20480\n"
        "ds_read_b64_tr_b16 a[28:29], v112 offset:6144\n"
        "ds_read_b64_tr_b16 a[30:31], v112 offset:22528\n"
        "ds_read_b64_tr_b16 a[88:89], v113 offset:4096\n"
        "ds_read_b64_tr_b16 a[90:91], v113 offset:20480\n"
        "ds_read_b64_tr_b16 a[92:93], v113 offset:6144\n"
        "ds_read_b64_tr_b16 a[94:95], v113 offset:22528\n"
        "s_waitcnt lgkmcnt(0)\n"
        "s_barrier\n"
        "s_add_u32 m0, s54, 65536\n"
        "s_add_u32 s53, s52, 2097152\n"
        "buffer_load_dwordx4 v105, s[68:71], s53 offen lds\n"
        "s_add_u32 m0, s54, 66560\n"
        "s_add_u32 s53, s52, 2162688\n"
        "buffer_load_dwordx4 v105, s[68:71], s53 offen lds\n"
        "s_add_u32 m0, s54, 67584\n"
        "s_add_u32 s53, s52, 2359296\n"
        "buffer_load_dwordx4 v105, s[68:71], s53 offen lds\n"
        "s_add_u32 m0, s54, 68608\n"
        "s_add_u32 s53, s52, 2424832\n"
        "buffer_load_dwordx4 v105, s[68:71], s53 offen lds\n"
        "s_add_u32 m0, s54, 69632\n"
        "s_add_u32 s53, s52, 2621440\n"
        "buffer_load_dwordx4 v105, s[68:71], s53 offen lds\n"
        "s_add_u32 m0, s54, 70656\n"
        "s_add_u32 s53, s52, 2686976\n"
        "buffer_load_dwordx4 v105, s[68:71], s53 offen lds\n"
        "s_add_u32 m0, s54, 71680\n"
        "s_add_u32 s53, s52, 2883584\n"
        "buffer_load_dwordx4 v105, s[68:71], s53 offen lds\n"
        "s_add_u32 m0, s54, 72704\n"
        "s_add_u32 s53, s52, 2949120\n"
        "buffer_load_dwordx4 v105, s[68:71], s53 offen lds\n"
        "s_waitcnt vmcnt(38)\n"
        "s_barrier\n"
        "ds_read_b64_tr_b16 a[32:33], v112 offset:32768\n"
        "ds_read_b64_tr_b16 a[34:35], v112 offset:49152\n"
        "ds_read_b64_tr_b16 a[36:37], v112 offset:34816\n"
        "ds_read_b64_tr_b16 a[38:39], v112 offset:51200\n"
        "ds_read_b64_tr_b16 a[96:97], v113 offset:32768\n"
        "ds_read_b64_tr_b16 a[98:99], v113 offset:49152\n"
        "ds_read_b64_tr_b16 a[100:101], v113 offset:34816\n"
        "ds_read_b64_tr_b16 a[102:103], v113 offset:51200\n"
        "ds_read_b64_tr_b16 a[40:41], v112 offset:36864\n"
        "ds_read_b64_tr_b16 a[42:43], v112 offset:53248\n"
        "ds_read_b64_tr_b16 a[44:45], v112 offset:38912\n"
        "ds_read_b64_tr_b16 a[46:47], v112 offset:55296\n"
        "ds_read_b64_tr_b16 a[104:105], v113 offset:36864\n"
        "ds_read_b64_tr_b16 a[106:107], v113 offset:53248\n"
        "ds_read_b64_tr_b16 a[108:109], v113 offset:38912\n"
        "ds_read_b64_tr_b16 a[110:111], v113 offset:55296\n"
        "s_waitcnt lgkmcnt(0)\n"
        "s_barrier\n"
        "s_add_u32 m0, s54, 98304\n"
        "s_add_u32 s53, s52, 3145728\n"
        "buffer_load_dwordx4 v105, s[68:71], s53 offen lds\n"
        "s_add_u32 m0, s54, 99328\n"
        "s_add_u32 s53, s52, 3211264\n"
        "buffer_load_dwordx4 v105, s[68:71], s53 offen lds\n"
        "s_add_u32 m0, s54, 100352\n"
        "s_add_u32 s53, s52, 3407872\n"
        "buffer_load_dwordx4 v105, s[68:71], s53 offen lds\n"
        "s_add_u32 m0, s54, 101376\n"
        "s_add_u32 s53, s52, 3473408\n"
        "buffer_load_dwordx4 v105, s[68:71], s53 offen lds\n"
        "s_add_u32 m0, s54, 102400\n"
        "s_add_u32 s53, s52, 3670016\n"
        "buffer_load_dwordx4 v105, s[68:71], s53 offen lds\n"
        "s_add_u32 m0, s54, 103424\n"
        "s_add_u32 s53, s52, 3735552\n"
        "buffer_load_dwordx4 v105, s[68:71], s53 offen lds\n"
        "s_add_u32 m0, s54, 104448\n"
        "s_add_u32 s53, s52, 3932160\n"
        "buffer_load_dwordx4 v105, s[68:71], s53 offen lds\n"
        "s_add_u32 m0, s54, 105472\n"
        "s_add_u32 s53, s52, 3997696\n"
        "buffer_load_dwordx4 v105, s[68:71], s53 offen lds\n"
        "s_waitcnt vmcnt(38)\n"
        "s_barrier\n"
        "ds_read_b64_tr_b16 a[48:49], v102 offset:0\n"
        "ds_read_b64_tr_b16 a[50:51], v102 offset:16384\n"
        "ds_read_b64_tr_b16 a[52:53], v102 offset:2048\n"
        "ds_read_b64_tr_b16 a[54:55], v102 offset:18432\n"
        "ds_read_b64_tr_b16 a[112:113], v103 offset:0\n"
        "ds_read_b64_tr_b16 a[114:115], v103 offset:16384\n"
        "ds_read_b64_tr_b16 a[116:117], v103 offset:2048\n"
        "ds_read_b64_tr_b16 a[118:119], v103 offset:18432\n"
        "ds_read_b64_tr_b16 a[56:57], v102 offset:4096\n"
        "ds_read_b64_tr_b16 a[58:59], v102 offset:20480\n"
        "ds_read_b64_tr_b16 a[60:61], v102 offset:6144\n"
        "ds_read_b64_tr_b16 a[62:63], v102 offset:22528\n"
        "ds_read_b64_tr_b16 a[120:121], v103 offset:4096\n"
        "ds_read_b64_tr_b16 a[122:123], v103 offset:20480\n"
        "ds_read_b64_tr_b16 a[124:125], v103 offset:6144\n"
        "ds_read_b64_tr_b16 a[126:127], v103 offset:22528\n"
        "s_waitcnt lgkmcnt(0)\n"
        "s_barrier\n"
        "s_mov_b32 s68, %[wvlo]\n"
        "s_and_b32 s69, %[wvhi], 0xffff\n"
        "s_add_u32 m0, s54, 131072\n"
        "s_add_u32 s53, s52, 0\n"
        "buffer_load_dwordx4 v105, s[68:71], s53 offen lds\n"
        "s_add_u32 m0, s54, 132096\n"
        "s_add_u32 s53, s52, 65536\n"
        "buffer_load_dwordx4 v105, s[68:71], s53 offen lds\n"
        "s_add_u32 m0, s54, 133120\n"
        "s_add_u32 s53, s52, 262144\n"
        "buffer_load_dwordx4 v105, s[68:71], s53 offen lds\n"
        "s_add_u32 m0, s54, 134144\n"
        "s_add_u32 s53, s52, 327680\n"
        "buffer_load_dwordx4 v105, s[68:71], s53 offen lds\n"
        "s_add_u32 m0, s54, 135168\n"
        "s_add_u32 s53, s52, 524288\n"
        "buffer_load_dwordx4 v105, s[68:71], s53 offen lds\n"
        "s_add_u32 m0, s54, 136192\n"
        "s_add_u32 s53, s52, 589824\n"
        "buffer_load_dwordx4 v105, s[68:71], s53 offen lds\n"
        "s_add_u32 m0, s54, 137216\n"
        "s_add_u32 s53, s52, 786432\n"
        "buffer_load_dwordx4 v105, s[68:71], s53 offen lds\n"
        "s_add_u32 m0, s54, 138240\n"
        "s_add_u32 s53, s52, 851968\n"
        "buffer_load_dwordx4 v105, s[68:71], s53 offen lds\n"
        "s_waitcnt vmcnt(38)\n"
        "s_barrier\n"
        "ds_read_b64_tr_b16 a[128:129], v110 offset:0\n"
        "ds_read_b64_tr_b16 a[130:131], v110 offset:16384\n"
        "ds_read_b64_tr_b16 a[132:133], v110 offset:2048\n"
        "ds_read_b64_tr_b16 a[134:135], v110 offset:18432\n"
        "ds_read_b64_tr_b16 a[192:193], v111 offset:0\n"
        "ds_read_b64_tr_b16 a[194:195], v111 offset:16384\n"
        "ds_read_b64_tr_b16 a[196:197], v111 offset:2048\n"
        "ds_read_b64_tr_b16 a[198:199], v111 offset:18432\n"
        "ds_read_b64_tr_b16 a[136:137], v110 offset:4096\n"
        "ds_read_b64_tr_b16 a[138:139], v110 offset:20480\n"
        "ds_read_b64_tr_b16 a[140:141], v110 offset:6144\n"
        "ds_read_b64_tr_b16 a[142:143], v110 offset:22528\n"
        "ds_read_b64_tr_b16 a[200:201], v111 offset:4096\n"
        "ds_read_b64_tr_b16 a[202:203], v111 offset:20480\n"
        "ds_read_b64_tr_b16 a[204:205], v111 offset:6144\n"
        "ds_read_b64_tr_b16 a[206:207], v111 offset:22528\n"
        "s_waitcnt lgkmcnt(0)\n"
        "s_barrier\n"
        "s_add_u32 m0, s54, 0\n"
        "s_add_u32 s53, s52, 1048576\n"
        "buffer_load_dwordx4 v105, s[68:71], s53 offen lds\n"
        "s_add_u32 m0, s54, 1024\n"
        "s_add_u32 s53, s52, 1114112\n"
        "buffer_load_dwordx4 v105, s[68:71], s53 offen lds\n"
        "s_add_u32 m0, s54, 2048\n"
        "s_add_u32 s53, s52, 1310720\n"
        "buffer_load_dwordx4 v105, s[68:71], s53 offen lds\n"
        "s_add_u32 m0, s54, 3072\n"
        "s_add_u32 s53, s52, 1376256\n"
        "buffer_load_dwordx4 v105, s[68:71], s53 offen lds\n"
        "s_add_u32 m0, s54, 4096\n"
        "s_add_u32 s53, s52, 1572864\n"
        "buffer_load_dwordx4 v105, s[68:71], s53 offen lds\n"
        "s_add_u32 m0, s54, 5120\n"
        "s_add_u32 s53, s52, 1638400\n"
        "buffer_load_dwordx4 v105, s[68:71], s53 offen lds\n"
        "s_add_u32 m0, s54, 6144\n"
        "s_add_u32 s53, s52, 1835008\n"
        "buffer_load_dwordx4 v105, s[68:71], s53 offen lds\n"
        "s_add_u32 m0, s54, 7168\n"
        "s_add_u32 s53, s52, 1900544\n"
        "buffer_load_dwordx4 v105, s[68:71], s53 offen lds\n"
        "s_waitcnt vmcnt(32)\n"
        "s_barrier\n"
        "ds_read_b64_tr_b16 a[144:145], v110 offset:32768\n"
        "ds_read_b64_tr_b16 a[146:147], v110 offset:49152\n"
        "ds_read_b64_tr_b16 a[148:149], v110 offset:34816\n"
        "ds_read_b64_tr_b16 a[150:151], v110 offset:51200\n"
        "ds_read_b64_tr_b16 a[208:209], v111 offset:32768\n"
        "ds_read_b64_tr_b16 a[210:211], v111 offset:49152\n"
        "ds_read_b64_tr_b16 a[212:213], v111 offset:34816\n"
        "ds_read_b64_tr_b16 a[214:215], v111 offset:51200\n"
        "ds_read_b64_tr_b16 a[152:153], v110 offset:36864\n"
        "ds_read_b64_tr_b16 a[154:155], v110 offset:53248\n"
        "ds_read_b64_tr_b16 a[156:157], v110 offset:38912\n"
        "ds_read_b64_tr_b16 a[158:159], v110 offset:55296\n"
        "ds_read_b64_tr_b16 a[216:217], v111 offset:36864\n"
        "ds_read_b64_tr_b16 a[218:219], v111 offset:53248\n"
        "ds_read_b64_tr_b16 a[220:221], v111 offset:38912\n"
        "ds_read_b64_tr_b16 a[222:223], v111 offset:55296\n"
        "s_waitcnt lgkmcnt(0)\n"
        "s_barrier\n"
        "s_add_u32 m0, s54, 32768\n"
        "s_add_u32 s53, s52, 2097152\n"
        "buffer_load_dwordx4 v105, s[68:71], s53 offen lds\n"
        "s_add_u32 m0, s54, 33792\n"
        "s_add_u32 s53, s52, 2162688\n"
        "buffer_load_dwordx4 v105, s[68:71], s53 offen lds\n"
        "s_add_u32 m0, s54, 34816\n"
        "s_add_u32 s53, s52, 2359296\n"
        "buffer_load_dwordx4 v105, s[68:71], s53 offen lds\n"
        "s_add_u32 m0, s54, 35840\n"
        "s_add_u32 s53, s52, 2424832\n"
        "buffer_load_dwordx4 v105, s[68:71], s53 offen lds\n"
        "s_add_u32 m0, s54, 36864\n"
        "s_add_u32 s53, s52, 2621440\n"
        "buffer_load_dwordx4 v105, s[68:71], s53 offen lds\n"
        "s_add_u32 m0, s54, 37888\n"
        "s_add_u32 s53, s52, 2686976\n"
        "buffer_load_dwordx4 v105, s[68:71], s53 offen lds\n"
        "s_add_u32 m0, s54, 38912\n"
        "s_add_u32 s53, s52, 2883584\n"
        "buffer_load_dwordx4 v105, s[68:71], s53 offen lds\n"
        "s_add_u32 m0, s54, 39936\n"
        "s_add_u32 s53, s52, 2949120\n"
        "buffer_load_dwordx4 v105, s[68:71], s53 offen lds\n"
        "s_waitcnt vmcnt(32)\n"
        "s_barrier\n"
        "ds_read_b64_tr_b16 a[160:161], v112 offset:0\n"
        "ds_read_b64_tr_b16 a[162:163], v112 offset:16384\n"
        "ds_read_b64_tr_b16 a[164:165], v112 offset:2048\n"
        "ds_read_b64_tr_b16 a[166:167], v112 offset:18432\n"
        "ds_read_b64_tr_b16 a[224:225], v113 offset:0\n"
        "ds_read_b64_tr_b16 a[226:227], v113 offset:16384\n"
        "ds_read_b64_tr_b16 a[228:229], v113 offset:2048\n"
        "ds_read_b64_tr_b16 a[230:231], v113 offset:18432\n"
        "ds_read_b64_tr_b16 a[168:169], v112 offset:4096\n"
        "ds_read_b64_tr_b16 a[170:171], v112 offset:20480\n"
        "ds_read_b64_tr_b16 a[172:173], v112 offset:6144\n"
        "ds_read_b64_tr_b16 a[174:175], v112 offset:22528\n"
        "ds_read_b64_tr_b16 a[232:233], v113 offset:4096\n"
        "ds_read_b64_tr_b16 a[234:235], v113 offset:20480\n"
        "ds_read_b64_tr_b16 a[236:237], v113 offset:6144\n"
        "ds_read_b64_tr_b16 a[238:239], v113 offset:22528\n"
        "s_waitcnt lgkmcnt(0)\n"
        "s_barrier\n"
        "s_add_u32 m0, s54, 65536\n"
        "s_add_u32 s53, s52, 3145728\n"
        "buffer_load_dwordx4 v105, s[68:71], s53 offen lds\n"
        "s_add_u32 m0, s54, 66560\n"
        "s_add_u32 s53, s52, 3211264\n"
        "buffer_load_dwordx4 v105, s[68:71], s53 offen lds\n"
        "s_add_u32 m0, s54, 67584\n"
        "s_add_u32 s53, s52, 3407872\n"
        "buffer_load_dwordx4 v105, s[68:71], s53 offen lds\n"
        "s_add_u32 m0, s54, 68608\n"
        "s_add_u32 s53, s52, 3473408\n"
        "buffer_load_dwordx4 v105, s[68:71], s53 offen lds\n"
        "s_add_u32 m0, s54, 69632\n"
        "s_add_u32 s53, s52, 3670016\n"
        "buffer_load_dwordx4 v105, s[68:71], s53 offen lds\n"
        "s_add_u32 m0, s54, 70656\n"
        "s_add_u32 s53, s52, 3735552\n"
        "buffer_load_dwordx4 v105, s[68:71], s53 offen lds\n"
        "s_add_u32 m0, s54, 71680\n"
        "s_add_u32 s53, s52, 3932160\n"
        "buffer_load_dwordx4 v105, s[68:71], s53 offen lds\n"
        "s_add_u32 m0, s54, 72704\n"
        "s_add_u32 s53, s52, 3997696\n"
        "buffer_load_dwordx4 v105, s[68:71], s53 offen lds\n"
        "s_waitcnt vmcnt(32)\n"
        "s_barrier\n"
        "ds_read_b64_tr_b16 a[176:177], v112 offset:32768\n"
        "ds_read_b64_tr_b16 a[178:179], v112 offset:49152\n"
        "ds_read_b64_tr_b16 a[180:181], v112 offset:34816\n"
        "ds_read_b64_tr_b16 a[182:183], v112 offset:51200\n"
        "ds_read_b64_tr_b16 a[240:241], v113 offset:32768\n"
        "ds_read_b64_tr_b16 a[242:243], v113 offset:49152\n"
        "ds_read_b64_tr_b16 a[244:245], v113 offset:34816\n"
        "ds_read_b64_tr_b16 a[246:247], v113 offset:51200\n"
        "ds_read_b64_tr_b16 a[184:185], v112 offset:36864\n"
        "ds_read_b64_tr_b16 a[186:187], v112 offset:53248\n"
        "ds_read_b64_tr_b16 a[188:189], v112 offset:38912\n"
        "ds_read_b64_tr_b16 a[190:191], v112 offset:55296\n"
        "ds_read_b64_tr_b16 a[248:249], v113 offset:36864\n"
        "ds_read_b64_tr_b16 a[250:251], v113 offset:53248\n"
        "ds_read_b64_tr_b16 a[252:253], v113 offset:38912\n"
        "ds_read_b64_tr_b16 a[254:255], v113 offset:55296\n"
        "s_waitcnt lgkmcnt(0)\n"
        "s_barrier\n"
        "s_mov_b32 s68, %[wolo]\n"
        "s_and_b32 s69, %[wohi], 0xffff\n"
        "s_lshl_b32 s52, s44, 17\n"
        "s_lshl_b32 s58, s79, 9\n"
        "s_add_u32 s52, s52, s58\n"
        "s_add_u32 m0, s54, 98304\n"
        "s_add_u32 s53, s52, 0\n"
        "buffer_load_dwordx4 v106, s[68:71], s53 offen lds\n"
        "s_add_u32 m0, s54, 99328\n"
        "s_add_u32 s53, s52, 8192\n"
        "buffer_load_dwordx4 v106, s[68:71], s53 offen lds\n"
        "s_add_u32 m0, s54, 100352\n"
        "s_add_u32 s53, s52, 32768\n"
        "buffer_load_dwordx4 v106, s[68:71], s53 offen lds\n"
        "s_add_u32 m0, s54, 101376\n"
        "s_add_u32 s53, s52, 40960\n"
        "buffer_load_dwordx4 v106, s[68:71], s53 offen lds\n"
        "s_add_u32 m0, s54, 102400\n"
        "s_add_u32 s53, s52, 65536\n"
        "buffer_load_dwordx4 v106, s[68:71], s53 offen lds\n"
        "s_add_u32 m0, s54, 103424\n"
        "s_add_u32 s53, s52, 73728\n"
        "buffer_load_dwordx4 v106, s[68:71], s53 offen lds\n"
        "s_add_u32 m0, s54, 104448\n"
        "s_add_u32 s53, s52, 98304\n"
        "buffer_load_dwordx4 v106, s[68:71], s53 offen lds\n"
        "s_add_u32 m0, s54, 105472\n"
        "s_add_u32 s53, s52, 106496\n"
        "buffer_load_dwordx4 v106, s[68:71], s53 offen lds\n"
        "s_waitcnt vmcnt(32)\n"
        "s_barrier\n"
        "ds_read_b64_tr_b16 v[128:129], v102 offset:0\n"
        "ds_read_b64_tr_b16 v[130:131], v102 offset:16384\n"
        "ds_read_b64_tr_b16 v[132:133], v102 offset:2048\n"
        "ds_read_b64_tr_b16 v[134:135], v102 offset:18432\n"
        "ds_read_b64_tr_b16 v[192:193], v103 offset:0\n"
        "ds_read_b64_tr_b16 v[194:195], v103 offset:16384\n"
        "ds_read_b64_tr_b16 v[196:197], v103 offset:2048\n"
        "ds_read_b64_tr_b16 v[198:199], v103 offset:18432\n"
        "ds_read_b64_tr_b16 v[136:137], v102 offset:4096\n"
        "ds_read_b64_tr_b16 v[138:139], v102 offset:20480\n"
        "ds_read_b64_tr_b16 v[140:141], v102 offset:6144\n"
        "ds_read_b64_tr_b16 v[142:143], v102 offset:22528\n"
        "ds_read_b64_tr_b16 v[200:201], v103 offset:4096\n"
        "ds_read_b64_tr_b16 v[202:203], v103 offset:20480\n"
        "ds_read_b64_tr_b16 v[204:205], v103 offset:6144\n"
        "ds_read_b64_tr_b16 v[206:207], v103 offset:22528\n"
        "s_waitcnt lgkmcnt(0)\n"
        "s_barrier\n"
        "s_add_u32 m0, s54, 131072\n"
        "s_add_u32 s53, s52, 256\n"
        "buffer_load_dwordx4 v106, s[68:71], s53 offen lds\n"
        "s_add_u32 m0, s54, 132096\n"
        "s_add_u32 s53, s52, 8448\n"
        "buffer_load_dwordx4 v106, s[68:71], s53 offen lds\n"
        "s_add_u32 m0, s54, 133120\n"
        "s_add_u32 s53, s52, 33024\n"
        "buffer_load_dwordx4 v106, s[68:71], s53 offen lds\n"
        "s_add_u32 m0, s54, 134144\n"
        "s_add_u32 s53, s52, 41216\n"
        "buffer_load_dwordx4 v106, s[68:71], s53 offen lds\n"
        "s_add_u32 m0, s54, 135168\n"
        "s_add_u32 s53, s52, 65792\n"
        "buffer_load_dwordx4 v106, s[68:71], s53 offen lds\n"
        "s_add_u32 m0, s54, 136192\n"
        "s_add_u32 s53, s52, 73984\n"
        "buffer_load_dwordx4 v106, s[68:71], s53 offen lds\n"
        "s_add_u32 m0, s54, 137216\n"
        "s_add_u32 s53, s52, 98560\n"
        "buffer_load_dwordx4 v106, s[68:71], s53 offen lds\n"
        "s_add_u32 m0, s54, 138240\n"
        "s_add_u32 s53, s52, 106752\n"
        "buffer_load_dwordx4 v106, s[68:71], s53 offen lds\n"
        "s_waitcnt vmcnt(32)\n"
        "s_barrier\n"
        "ds_read_b64_tr_b16 v[144:145], v110 offset:0\n"
        "ds_read_b64_tr_b16 v[146:147], v110 offset:16384\n"
        "ds_read_b64_tr_b16 v[148:149], v110 offset:2048\n"
        "ds_read_b64_tr_b16 v[150:151], v110 offset:18432\n"
        "ds_read_b64_tr_b16 v[208:209], v111 offset:0\n"
        "ds_read_b64_tr_b16 v[210:211], v111 offset:16384\n"
        "ds_read_b64_tr_b16 v[212:213], v111 offset:2048\n"
        "ds_read_b64_tr_b16 v[214:215], v111 offset:18432\n"
        "ds_read_b64_tr_b16 v[152:153], v110 offset:4096\n"
        "ds_read_b64_tr_b16 v[154:155], v110 offset:20480\n"
        "ds_read_b64_tr_b16 v[156:157], v110 offset:6144\n"
        "ds_read_b64_tr_b16 v[158:159], v110 offset:22528\n"
        "ds_read_b64_tr_b16 v[216:217], v111 offset:4096\n"
        "ds_read_b64_tr_b16 v[218:219], v111 offset:20480\n"
        "ds_read_b64_tr_b16 v[220:221], v111 offset:6144\n"
        "ds_read_b64_tr_b16 v[222:223], v111 offset:22528\n"
        "s_waitcnt lgkmcnt(0)\n"
        "s_barrier\n"
        "s_waitcnt vmcnt(24)\n"
        "s_barrier\n"
        "ds_read_b64_tr_b16 v[160:161], v110 offset:32768\n"
        "ds_read_b64_tr_b16 v[162:163], v110 offset:49152\n"
        "ds_read_b64_tr_b16 v[164:165], v110 offset:34816\n"
        "ds_read_b64_tr_b16 v[166:167], v110 offset:51200\n"
        "ds_read_b64_tr_b16 v[224:225], v111 offset:32768\n"
        "ds_read_b64_tr_b16 v[226:227], v111 offset:49152\n"
        "ds_read_b64_tr_b16 v[228:229], v111 offset:34816\n"
        "ds_read_b64_tr_b16 v[230:231], v111 offset:51200\n"
        "ds_read_b64_tr_b16 v[168:169], v110 offset:36864\n"
        "ds_read_b64_tr_b16 v[170:171], v110 offset:53248\n"
        "ds_read_b64_tr_b16 v[172:173], v110 offset:38912\n"
        "ds_read_b64_tr_b16 v[174:175], v110 offset:55296\n"
        "ds_read_b64_tr_b16 v[232:233], v111 offset:36864\n"
        "ds_read_b64_tr_b16 v[234:235], v111 offset:53248\n"
        "ds_read_b64_tr_b16 v[236:237], v111 offset:38912\n"
        "ds_read_b64_tr_b16 v[238:239], v111 offset:55296\n"
        "s_waitcnt lgkmcnt(0)\n"
        "s_barrier\n"
        "s_waitcnt vmcnt(16)\n"
        "s_barrier\n"
        "ds_write_b128 v119, v[120:123] offset:0\n"
        "ds_write_b128 v119, v[124:127] offset:4096\n"
        "ds_write_b128 v0, v[92:95] offset:16384\n"
        "ds_write_b128 v0, v[96:99] offset:17408\n"
        "v_readfirstlane_b32 s47, v19\n"
        "s_add_u32 s47, s47, 1\n"
        "buffer_load_dwordx2 v[16:17], v12, s[40:43], s77 offen sc1\n"
        "ds_read_b64_tr_b16 v[176:177], v112 offset:0\n"
        "ds_read_b64_tr_b16 v[178:179], v112 offset:16384\n"
        "ds_read_b64_tr_b16 v[180:181], v112 offset:2048\n"
        "ds_read_b64_tr_b16 v[182:183], v112 offset:18432\n"
        "ds_read_b64_tr_b16 v[240:241], v113 offset:0\n"
        "ds_read_b64_tr_b16 v[242:243], v113 offset:16384\n"
        "ds_read_b64_tr_b16 v[244:245], v113 offset:2048\n"
        "ds_read_b64_tr_b16 v[246:247], v113 offset:18432\n"
        "ds_read_b64_tr_b16 v[184:185], v112 offset:4096\n"
        "ds_read_b64_tr_b16 v[186:187], v112 offset:20480\n"
        "ds_read_b64_tr_b16 v[188:189], v112 offset:6144\n"
        "ds_read_b64_tr_b16 v[190:191], v112 offset:22528\n"
        "ds_read_b64_tr_b16 v[248:249], v113 offset:4096\n"
        "ds_read_b64_tr_b16 v[250:251], v113 offset:20480\n"
        "ds_read_b64_tr_b16 v[252:253], v113 offset:6144\n"
        "ds_read_b64_tr_b16 v[254:255], v113 offset:22528\n"
        "s_waitcnt lgkmcnt(0)\n"
        "s_barrier\n"
        "s_waitcnt vmcnt(9)\n"
        "s_barrier\n"
        "ds_read_b64_tr_b16 v[24:25], v112 offset:32768\n"
        "ds_read_b64_tr_b16 v[26:27], v112 offset:49152\n"
        "ds_read_b64_tr_b16 v[28:29], v112 offset:34816\n"
        "ds_read_b64_tr_b16 v[30:31], v112 offset:51200\n"
        "ds_read_b64_tr_b16 v[40:41], v113 offset:32768\n"
        "ds_read_b64_tr_b16 v[42:43], v113 offset:49152\n"
        "ds_read_b64_tr_b16 v[44:45], v113 offset:34816\n"
        "ds_read_b64_tr_b16 v[46:47], v113 offset:51200\n"
        "ds_read_b64_tr_b16 v[32:33], v112 offset:36864\n"
        "ds_read_b64_tr_b16 v[34:35], v112 offset:53248\n"
        "ds_read_b64_tr_b16 v[36:37], v112 offset:38912\n"
        "ds_read_b64_tr_b16 v[38:39], v112 offset:55296\n"
        "ds_read_b64_tr_b16 v[48:49], v113 offset:36864\n"
        "ds_read_b64_tr_b16 v[50:51], v113 offset:53248\n"
        "ds_read_b64_tr_b16 v[52:53], v113 offset:38912\n"
        "ds_read_b64_tr_b16 v[54:55], v113 offset:55296\n"
        "s_waitcnt lgkmcnt(0)\n"
        "s_barrier\n"
        "ds_write_b128 v2, v[24:27] offset:0\n"
        "ds_write_b128 v2, v[28:31] offset:1024\n"
        "ds_write_b128 v2, v[32:35] offset:2048\n"
        "ds_write_b128 v2, v[36:39] offset:3072\n"
        "ds_write_b128 v2, v[40:43] offset:4096\n"
        "ds_write_b128 v2, v[44:47] offset:5120\n"
        "ds_write_b128 v2, v[48:51] offset:6144\n"
        "ds_write_b128 v2, v[52:55] offset:7168\n"
        "s_waitcnt lgkmcnt(0)\n"
        "s_waitcnt vmcnt(0)\n"
        "s_barrier\n"
        "ds_read_b64_tr_b16 v[24:25], v102 offset:0\n"
        "ds_read_b64_tr_b16 v[26:27], v102 offset:16384\n"
        "ds_read_b64_tr_b16 v[28:29], v102 offset:2048\n"
        "ds_read_b64_tr_b16 v[30:31], v102 offset:18432\n"
        "ds_read_b64_tr_b16 v[40:41], v103 offset:0\n"
        "ds_read_b64_tr_b16 v[42:43], v103 offset:16384\n"
        "ds_read_b64_tr_b16 v[44:45], v103 offset:2048\n"
        "ds_read_b64_tr_b16 v[46:47], v103 offset:18432\n"
        "ds_read_b64_tr_b16 v[32:33], v102 offset:4096\n"
        "ds_read_b64_tr_b16 v[34:35], v102 offset:20480\n"
        "ds_read_b64_tr_b16 v[36:37], v102 offset:6144\n"
        "ds_read_b64_tr_b16 v[38:39], v102 offset:22528\n"
        "ds_read_b64_tr_b16 v[48:49], v103 offset:4096\n"
        "ds_read_b64_tr_b16 v[50:51], v103 offset:20480\n"
        "ds_read_b64_tr_b16 v[52:53], v103 offset:6144\n"
        "ds_read_b64_tr_b16 v[54:55], v103 offset:22528\n"
        "s_waitcnt lgkmcnt(0)\n"
        "s_barrier\n"
        "ds_write_b128 v2, v[24:27] offset:32768\n"
        "ds_write_b128 v2, v[28:31] offset:33792\n"
        "ds_write_b128 v2, v[32:35] offset:34816\n"
        "ds_write_b128 v2, v[36:39] offset:35840\n"
        "ds_write_b128 v2, v[40:43] offset:36864\n"
        "ds_write_b128 v2, v[44:47] offset:37888\n"
        "ds_write_b128 v2, v[48:51] offset:38912\n"
        "ds_write_b128 v2, v[52:55] offset:39936\n"
        "s_waitcnt lgkmcnt(0)\n"
        "s_branch LH_G2\n"
        "LH_G1:\n"
        "s_waitcnt vmcnt(38)\n"
        "s_barrier\n"
        "ds_read_b64_tr_b16 a[160:161], v110 offset:32768\n"
        "ds_read_b64_tr_b16 a[162:163], v110 offset:49152\n"
        "ds_read_b64_tr_b16 a[164:165], v110 offset:34816\n"
        "ds_read_b64_tr_b16 a[166:167], v110 offset:51200\n"
        "ds_read_b64_tr_b16 a[224:225], v111 offset:32768\n"
        "ds_read_b64_tr_b16 a[226:227], v111 offset:49152\n"
        "ds_read_b64_tr_b16 a[228:229], v111 offset:34816\n"
        "ds_read_b64_tr_b16 a[230:231], v111 offset:51200\n"
        "ds_read_b64_tr_b16 a[168:169], v110 offset:36864\n"
        "ds_read_b64_tr_b16 a[170:171], v110 offset:53248\n"
        "ds_read_b64_tr_b16 a[172:173], v110 offset:38912\n"
        "ds_read_b64_tr_b16 a[174:175], v110 offset:55296\n"
        "ds_read_b64_tr_b16 a[232:233], v111 offset:36864\n"
        "ds_read_b64_tr_b16 a[234:235], v111 offset:53248\n"
        "ds_read_b64_tr_b16 a[236:237], v111 offset:38912\n"
        "ds_read_b64_tr_b16 a[238:239], v111 offset:55296\n"
        "s_waitcnt lgkmcnt(0)\n"
        "s_barrier\n"
        "s_add_u32 m0, s54, 32768\n"
        "s_add_u32 s53, s52, 3145728\n"
        "buffer_load_dwordx4 v105, s[68:71], s53 offen lds\n"
        "s_add_u32 m0, s54, 33792\n"
        "s_add_u32 s53, s52, 3211264\n"
        "buffer_load_dwordx4 v105, s[68:71], s53 offen lds\n"
        "s_add_u32 m0, s54, 34816\n"
        "s_add_u32 s53, s52, 3407872\n"
        "buffer_load_dwordx4 v105, s[68:71], s53 offen lds\n"
        "s_add_u32 m0, s54, 35840\n"
        "s_add_u32 s53, s52, 3473408\n"
        "buffer_load_dwordx4 v105, s[68:71], s53 offen lds\n"
        "s_add_u32 m0, s54, 36864\n"
        "s_add_u32 s53, s52, 3670016\n"
        "buffer_load_dwordx4 v105, s[68:71], s53 offen lds\n"
        "s_add_u32 m0, s54, 37888\n"
        "s_add_u32 s53, s52, 3735552\n"
        "buffer_load_dwordx4 v105, s[68:71], s53 offen lds\n"
        "s_add_u32 m0, s54, 38912\n"
        "s_add_u32 s53, s52, 3932160\n"
        "buffer_load_dwordx4 v105, s[68:71], s53 offen lds\n"
        "s_add_u32 m0, s54, 39936\n"
        "s_add_u32 s53, s52, 3997696\n"
        "buffer_load_dwordx4 v105, s[68:71], s53 offen lds\n"
        "s_waitcnt vmcnt(38)\n"
        "s_barrier\n"
        "ds_read_b64_tr_b16 a[176:177], v112 offset:0\n"
        "ds_read_b64_tr_b16 a[178:179], v112 offset:16384\n"
        "ds_read_b64_tr_b16 a[180:181], v112 offset:2048\n"
        "ds_read_b64_tr_b16 a[182:183], v112 offset:18432\n"
        "ds_read_b64_tr_b16 a[240:241], v113 offset:0\n"
        "ds_read_b64_tr_b16 a[242:243], v113 offset:16384\n"
        "ds_read_b64_tr_b16 a[244:245], v113 offset:2048\n"
        "ds_read_b64_tr_b16 a[246:247], v113 offset:18432\n"
        "ds_read_b64_tr_b16 a[184:185], v112 offset:4096\n"
        "ds_read_b64_tr_b16 a[186:187], v112 offset:20480\n"
        "ds_read_b64_tr_b16 a[188:189], v112 offset:6144\n"
        "ds_read_b64_tr_b16 a[190:191], v112 offset:22528\n"
        "ds_read_b64_tr_b16 a[248:249], v113 offset:4096\n"
        "ds_read_b64_tr_b16 a[250:251], v113 offset:20480\n"
        "ds_read_b64_tr_b16 a[252:253], v113 offset:6144\n"
        "ds_read_b64_tr_b16 a[254:255], v113 offset:22528\n"
        "s_waitcnt lgkmcnt(0)\n"
        "s_barrier\n"
        "s_mov_b32 s68, %[wqlo]\n"
        "s_and_b32 s69, %[wqhi], 0xffff\n"
        "s_add_u32 m0, s54, 65536\n"
        "s_add_u32 s53, s52, 0\n"
        "buffer_load_dwordx4 v105, s[68:71], s53 offen lds\n"
        "s_add_u32 m0, s54, 66560\n"
        "s_add_u32 s53, s52, 65536\n"
        "buffer_load_dwordx4 v105, s[68:71], s53 offen lds\n"
        "s_add_u32 m0, s54, 67584\n"
        "s_add_u32 s53, s52, 262144\n"
        "buffer_load_dwordx4 v105, s[68:71], s53 offen lds\n"
        "s_add_u32 m0, s54, 68608\n"
        "s_add_u32 s53, s52, 327680\n"
        "buffer_load_dwordx4 v105, s[68:71], s53 offen lds\n"
        "s_add_u32 m0, s54, 69632\n"
        "s_add_u32 s53, s52, 524288\n"
        "buffer_load_dwordx4 v105, s[68:71], s53 offen lds\n"
        "s_add_u32 m0, s54, 70656\n"
        "s_add_u32 s53, s52, 589824\n"
        "buffer_load_dwordx4 v105, s[68:71], s53 offen lds\n"
        "s_add_u32 m0, s54, 71680\n"
        "s_add_u32 s53, s52, 786432\n"
        "buffer_load_dwordx4 v105, s[68:71], s53 offen lds\n"
        "s_add_u32 m0, s54, 72704\n"
        "s_add_u32 s53, s52, 851968\n"
        "buffer_load_dwordx4 v105, s[68:71], s53 offen lds\n"
        "s_waitcnt vmcnt(38)\n"
        "s_barrier\n"
        "ds_read_b64_tr_b16 v[128:129], v112 offset:32768\n"
        "ds_read_b64_tr_b16 v[130:131], v112 offset:49152\n"
        "ds_read_b64_tr_b16 v[132:133], v112 offset:34816\n"
        "ds_read_b64_tr_b16 v[134:135], v112 offset:51200\n"
        "ds_read_b64_tr_b16 v[192:193], v113 offset:32768\n"
        "ds_read_b64_tr_b16 v[194:195], v113 offset:49152\n"
        "ds_read_b64_tr_b16 v[196:197], v113 offset:34816\n"
        "ds_read_b64_tr_b16 v[198:199], v113 offset:51200\n"
        "ds_read_b64_tr_b16 v[136:137], v112 offset:36864\n"
        "ds_read_b64_tr_b16 v[138:139], v112 offset:53248\n"
        "ds_read_b64_tr_b16 v[140:141], v112 offset:38912\n"
        "ds_read_b64_tr_b16 v[142:143], v112 offset:55296\n"
        "ds_read_b64_tr_b16 v[200:201], v113 offset:36864\n"
        "ds_read_b64_tr_b16 v[202:203], v113 offset:53248\n"
        "ds_read_b64_tr_b16 v[204:205], v113 offset:38912\n"
        "ds_read_b64_tr_b16 v[206:207], v113 offset:55296\n"
        "s_waitcnt lgkmcnt(0)\n"
        "s_barrier\n"
        "s_add_u32 m0, s54, 98304\n"
        "s_add_u32 s53, s52, 1048576\n"
        "buffer_load_dwordx4 v105, s[68:71], s53 offen lds\n"
        "s_add_u32 m0, s54, 99328\n"
        "s_add_u32 s53, s52, 1114112\n"
        "buffer_load_dwordx4 v105, s[68:71], s53 offen lds\n"
        "s_add_u32 m0, s54, 100352\n"
        "s_add_u32 s53, s52, 1310720\n"
        "buffer_load_dwordx4 v105, s[68:71], s53 offen lds\n"
        "s_add_u32 m0, s54, 101376\n"
        "s_add_u32 s53, s52, 1376256\n"
        "buffer_load_dwordx4 v105, s[68:71], s53 offen lds\n"
        "s_add_u32 m0, s54, 102400\n"
        "s_add_u32 s53, s52, 1572864\n"
        "buffer_load_dwordx4 v105, s[68:71], s53 offen lds\n"
        "s_add_u32 m0, s54, 103424\n"
        "s_add_u32 s53, s52, 1638400\n"
        "buffer_load_dwordx4 v105, s[68:71], s53 offen lds\n"
        "s_add_u32 m0, s54, 104448\n"
        "s_add_u32 s53, s52, 1835008\n"
        "buffer_load_dwordx4 v105, s[68:71], s53 offen lds\n"
        "s_add_u32 m0, s54, 105472\n"
        "s_add_u32 s53, s52, 1900544\n"
        "buffer_load_dwordx4 v105, s[68:71], s53 offen lds\n"
        "s_waitcnt vmcnt(38)\n"
        "s_barrier\n"
        "ds_read_b64_tr_b16 v[144:145], v102 offset:0\n"
        "ds_read_b64_tr_b16 v[146:147], v102 offset:16384\n"
        "ds_read_b64_tr_b16 v[148:149], v102 offset:2048\n"
        "ds_read_b64_tr_b16 v[150:151], v102 offset:18432\n"
        "ds_read_b64_tr_b16 v[208:209], v103 offset:0\n"
        "ds_read_b64_tr_b16 v[210:211], v103 offset:16384\n"
        "ds_read_b64_tr_b16 v[212:213], v103 offset:2048\n"
        "ds_read_b64_tr_b16 v[214:215], v103 offset:18432\n"
        "ds_read_b64_tr_b16 v[152:153], v102 offset:4096\n"
        "ds_read_b64_tr_b16 v[154:155], v102 offset:20480\n"
        "ds_read_b64_tr_b16 v[156:157], v102 offset:6144\n"
        "ds_read_b64_tr_b16 v[158:159], v102 offset:22528\n"
        "ds_read_b64_tr_b16 v[216:217], v103 offset:4096\n"
        "ds_read_b64_tr_b16 v[218:219], v103 offset:20480\n"
        "ds_read_b64_tr_b16 v[220:221], v103 offset:6144\n"
        "ds_read_b64_tr_b16 v[222:223], v103 offset:22528\n"
        "s_waitcnt lgkmcnt(0)\n"
        "s_barrier\n"
        "s_add_u32 m0, s54, 131072\n"
        "s_add_u32 s53, s52, 2097152\n"
        "buffer_load_dwordx4 v105, s[68:71], s53 offen lds\n"
        "s_add_u32 m0, s54, 132096\n"
        "s_add_u32 s53, s52, 2162688\n"
        "buffer_load_dwordx4 v105, s[68:71], s53 offen lds\n"
        "s_add_u32 m0, s54, 133120\n"
        "s_add_u32 s53, s52, 2359296\n"
        "buffer_load_dwordx4 v105, s[68:71], s53 offen lds\n"
        "s_add_u32 m0, s54, 134144\n"
        "s_add_u32 s53, s52, 2424832\n"
        "buffer_load_dwordx4 v105, s[68:71], s53 offen lds\n"
        "s_add_u32 m0, s54, 135168\n"
        "s_add_u32 s53, s52, 2621440\n"
        "buffer_load_dwordx4 v105, s[68:71], s53 offen lds\n"
        "s_add_u32 m0, s54, 136192\n"
        "s_add_u32 s53, s52, 2686976\n"
        "buffer_load_dwordx4 v105, s[68:71], s53 offen lds\n"
        "s_add_u32 m0, s54, 137216\n"
        "s_add_u32 s53, s52, 2883584\n"
        "buffer_load_dwordx4 v105, s[68:71], s53 offen lds\n"
        "s_add_u32 m0, s54, 138240\n"
        "s_add_u32 s53, s52, 2949120\n"
        "buffer_load_dwordx4 v105, s[68:71], s53 offen lds\n"
        "s_waitcnt vmcnt(38)\n"
        "s_barrier\n"
        "ds_read_b64_tr_b16 v[160:161], v110 offset:0\n"
        "ds_read_b64_tr_b16 v[162:163], v110 offset:16384\n"
        "ds_read_b64_tr_b16 v[164:165], v110 offset:2048\n"
        "ds_read_b64_tr_b16 v[166:167], v110 offset:18432\n"
        "ds_read_b64_tr_b16 v[224:225], v111 offset:0\n"
        "ds_read_b64_tr_b16 v[226:227], v111 offset:16384\n"
        "ds_read_b64_tr_b16 v[228:229], v111 offset:2048\n"
        "ds_read_b64_tr_b16 v[230:231], v111 offset:18432\n"
        "ds_read_b64_tr_b16 v[168:169], v110 offset:4096\n"
        "ds_read_b64_tr_b16 v[170:171], v110 offset:20480\n"
        "ds_read_b64_tr_b16 v[172:173], v110 offset:6144\n"
        "ds_read_b64_tr_b16 v[174:175], v110 offset:22528\n"
        "ds_read_b64_tr_b16 v[232:233], v111 offset:4096\n"
        "ds_read_b64_tr_b16 v[234:235], v111 offset:20480\n"
        "ds_read_b64_tr_b16 v[236:237], v111 offset:6144\n"
        "ds_read_b64_tr_b16 v[238:239], v111 offset:22528\n"
        "s_waitcnt lgkmcnt(0)\n"
        "s_barrier\n"
        "s_add_u32 m0, s54, 0\n"
        "s_add_u32 s53, s52, 3145728\n"
        "buffer_load_dwordx4 v105, s[68:71], s53 offen lds\n"
        "s_add_u32 m0, s54, 1024\n"
        "s_add_u32 s53, s52, 3211264\n"
        "buffer_load_dwordx4 v105, s[68:71], s53 offen lds\n"
        "s_add_u32 m0, s54, 2048\n"
        "s_add_u32 s53, s52, 3407872\n"
        "buffer_load_dwordx4 v105, s[68:71], s53 offen lds\n"
        "s_add_u32 m0, s54, 3072\n"
        "s_add_u32 s53, s52, 3473408\n"
        "buffer_load_dwordx4 v105, s[68:71], s53 offen lds\n"
        "s_add_u32 m0, s54, 4096\n"
        "s_add_u32 s53, s52, 3670016\n"
        "buffer_load_dwordx4 v105, s[68:71], s53 offen lds\n"
        "s_add_u32 m0, s54, 5120\n"
        "s_add_u32 s53, s52, 3735552\n"
        "buffer_load_dwordx4 v105, s[68:71], s53 offen lds\n"
        "s_add_u32 m0, s54, 6144\n"
        "s_add_u32 s53, s52, 3932160\n"
        "buffer_load_dwordx4 v105, s[68:71], s53 offen lds\n"
        "s_add_u32 m0, s54, 7168\n"
        "s_add_u32 s53, s52, 3997696\n"
        "buffer_load_dwordx4 v105, s[68:71], s53 offen lds\n"
        "s_waitcnt vmcnt(32)\n"
        "s_barrier\n"
        "ds_read_b64_tr_b16 v[176:177], v110 offset:32768\n"
        "ds_read_b64_tr_b16 v[178:179], v110 offset:49152\n"
        "ds_read_b64_tr_b16 v[180:181], v110 offset:34816\n"
        "ds_read_b64_tr_b16 v[182:183], v110 offset:51200\n"
        "ds_read_b64_tr_b16 v[240:241], v111 offset:32768\n"
        "ds_read_b64_tr_b16 v[242:243], v111 offset:49152\n"
        "ds_read_b64_tr_b16 v[244:245], v111 offset:34816\n"
        "ds_read_b64_tr_b16 v[246:247], v111 offset:51200\n"
        "ds_read_b64_tr_b16 v[184:185], v110 offset:36864\n"
        "ds_read_b64_tr_b16 v[186:187], v110 offset:53248\n"
        "ds_read_b64_tr_b16 v[188:189], v110 offset:38912\n"
        "ds_read_b64_tr_b16 v[190:191], v110 offset:55296\n"
        "ds_read_b64_tr_b16 v[248:249], v111 offset:36864\n"
        "ds_read_b64_tr_b16 v[250:251], v111 offset:53248\n"
        "ds_read_b64_tr_b16 v[252:253], v111 offset:38912\n"
        "ds_read_b64_tr_b16 v[254:255], v111 offset:55296\n"
        "s_waitcnt lgkmcnt(0)\n"
        "s_barrier\n"
        "s_mov_b32 s68, %[wklo]\n"
        "s_and_b32 s69, %[wkhi], 0xffff\n"
        "s_add_u32 m0, s54, 32768\n"
        "s_add_u32 s53, s52, 0\n"
        "buffer_load_dwordx4 v105, s[68:71], s53 offen lds\n"
        "s_add_u32 m0, s54, 33792\n"
        "s_add_u32 s53, s52, 65536\n"
        "buffer_load_dwordx4 v105, s[68:71], s53 offen lds\n"
        "s_add_u32 m0, s54, 34816\n"
        "s_add_u32 s53, s52, 262144\n"
        "buffer_load_dwordx4 v105, s[68:71], s53 offen lds\n"
        "s_add_u32 m0, s54, 35840\n"
        "s_add_u32 s53, s52, 327680\n"
        "buffer_load_dwordx4 v105, s[68:71], s53 offen lds\n"
        "s_add_u32 m0, s54, 36864\n"
        "s_add_u32 s53, s52, 524288\n"
        "buffer_load_dwordx4 v105, s[68:71], s53 offen lds\n"
        "s_add_u32 m0, s54, 37888\n"
        "s_add_u32 s53, s52, 589824\n"
        "buffer_load_dwordx4 v105, s[68:71], s53 offen lds\n"
        "s_add_u32 m0, s54, 38912\n"
        "s_add_u32 s53, s52, 786432\n"
        "buffer_load_dwordx4 v105, s[68:71], s53 offen lds\n"
        "s_add_u32 m0, s54, 39936\n"
        "s_add_u32 s53, s52, 851968\n"
        "buffer_load_dwordx4 v105, s[68:71], s53 offen lds\n"
        "s_waitcnt vmcnt(32)\n"
        "s_barrier\n"
        "ds_read_b64_tr_b16 a[0:1], v112 offset:0\n"
        "ds_read_b64_tr_b16 a[2:3], v112 offset:16384\n"
        "ds_read_b64_tr_b16 a[4:5], v112 offset:2048\n"
        "ds_read_b64_tr_b16 a[6:7], v112 offset:18432\n"
        "ds_read_b64_tr_b16 a[64:65], v113 offset:0\n"
        "ds_read_b64_tr_b16 a[66:67], v113 offset:16384\n"
        "ds_read_b64_tr_b16 a[68:69], v113 offset:2048\n"
        "ds_read_b64_tr_b16 a[70:71], v113 offset:18432\n"
        "ds_read_b64_tr_b16 a[8:9], v112 offset:4096\n"
        "ds_read_b64_tr_b16 a[10:11], v112 offset:20480\n"
        "ds_read_b64_tr_b16 a[12:13], v112 offset:6144\n"
        "ds_read_b64_tr_b16 a[14:15], v112 offset:22528\n"
        "ds_read_b64_tr_b16 a[72:73], v113 offset:4096\n"
        "ds_read_b64_tr_b16 a[74:75], v113 offset:20480\n"
        "ds_read_b64_tr_b16 a[76:77], v113 offset:6144\n"
        "ds_read_b64_tr_b16 a[78:79], v113 offset:22528\n"
        "s_waitcnt lgkmcnt(0)\n"
        "s_barrier\n"
        "s_add_u32 m0, s54, 65536\n"
        "s_add_u32 s53, s52, 1048576\n"
        "buffer_load_dwordx4 v105, s[68:71], s53 offen lds\n"
        "s_add_u32 m0, s54, 66560\n"
        "s_add_u32 s53, s52, 1114112\n"
        "buffer_load_dwordx4 v105, s[68:71], s53 offen lds\n"
        "s_add_u32 m0, s54, 67584\n"
        "s_add_u32 s53, s52, 1310720\n"
        "buffer_load_dwordx4 v105, s[68:71], s53 offen lds\n"
        "s_add_u32 m0, s54, 68608\n"
        "s_add_u32 s53, s52, 1376256\n"
        "buffer_load_dwordx4 v105, s[68:71], s53 offen lds\n"
        "s_add_u32 m0, s54, 69632\n"
        "s_add_u32 s53, s52, 1572864\n"
        "buffer_load_dwordx4 v105, s[68:71], s53 offen lds\n"
        "s_add_u32 m0, s54, 70656\n"
        "s_add_u32 s53, s52, 1638400\n"
        "buffer_load_dwordx4 v105, s[68:71], s53 offen lds\n"
        "s_add_u32 m0, s54, 71680\n"
        "s_add_u32 s53, s52, 1835008\n"
        "buffer_load_dwordx4 v105, s[68:71], s53 offen lds\n"
        "s_add_u32 m0, s54, 72704\n"
        "s_add_u32 s53, s52, 1900544\n"
        "buffer_load_dwordx4 v105, s[68:71], s53 offen lds\n"
        "s_waitcnt vmcnt(32)\n"
        "s_barrier\n"
        "ds_read_b64_tr_b16 a[16:17], v112 offset:32768\n"
        "ds_read_b64_tr_b16 a[18:19], v112 offset:49152\n"
        "ds_read_b64_tr_b16 a[20:21], v112 offset:34816\n"
        "ds_read_b64_tr_b16 a[22:23], v112 offset:51200\n"
        "ds_read_b64_tr_b16 a[80:81], v113 offset:32768\n"
        "ds_read_b64_tr_b16 a[82:83], v113 offset:49152\n"
        "ds_read_b64_tr_b16 a[84:85], v113 offset:34816\n"
        "ds_read_b64_tr_b16 a[86:87], v113 offset:51200\n"
        "ds_read_b64_tr_b16 a[24:25], v112 offset:36864\n"
        "ds_read_b64_tr_b16 a[26:27], v112 offset:53248\n"
        "ds_read_b64_tr_b16 a[28:29], v112 offset:38912\n"
        "ds_read_b64_tr_b16 a[30:31], v112 offset:55296\n"
        "ds_read_b64_tr_b16 a[88:89], v113 offset:36864\n"
        "ds_read_b64_tr_b16 a[90:91], v113 offset:53248\n"
        "ds_read_b64_tr_b16 a[92:93], v113 offset:38912\n"
        "ds_read_b64_tr_b16 a[94:95], v113 offset:55296\n"
        "s_waitcnt lgkmcnt(0)\n"
        "s_barrier\n"
        "s_mov_b32 s68, %[wolo]\n"
        "s_and_b32 s69, %[wohi], 0xffff\n"
        "s_lshl_b32 s52, s44, 17\n"
        "s_lshl_b32 s58, s79, 9\n"
        "s_add_u32 s52, s52, s58\n"
        "s_add_u32 m0, s54, 98304\n"
        "s_add_u32 s53, s52, 0\n"
        "buffer_load_dwordx4 v106, s[68:71], s53 offen lds\n"
        "s_add_u32 m0, s54, 99328\n"
        "s_add_u32 s53, s52, 8192\n"
        "buffer_load_dwordx4 v106, s[68:71], s53 offen lds\n"
        "s_add_u32 m0, s54, 100352\n"
        "s_add_u32 s53, s52, 32768\n"
        "buffer_load_dwordx4 v106, s[68:71], s53 offen lds\n"
        "s_add_u32 m0, s54, 101376\n"
        "s_add_u32 s53, s52, 40960\n"
        "buffer_load_dwordx4 v106, s[68:71], s53 offen lds\n"
        "s_add_u32 m0, s54, 102400\n"
        "s_add_u32 s53, s52, 65536\n"
        "buffer_load_dwordx4 v106, s[68:71], s53 offen lds\n"
        "s_add_u32 m0, s54, 103424\n"
        "s_add_u32 s53, s52, 73728\n"
        "buffer_load_dwordx4 v106, s[68:71], s53 offen lds\n"
        "s_add_u32 m0, s54, 104448\n"
        "s_add_u32 s53, s52, 98304\n"
        "buffer_load_dwordx4 v106, s[68:71], s53 offen lds\n"
        "s_add_u32 m0, s54, 105472\n"
        "s_add_u32 s53, s52, 106496\n"
        "buffer_load_dwordx4 v106, s[68:71], s53 offen lds\n"
        "s_waitcnt vmcnt(32)\n"
        "s_barrier\n"
        "ds_read_b64_tr_b16 a[32:33], v102 offset:0\n"
        "ds_read_b64_tr_b16 a[34:35], v102 offset:16384\n"
        "ds_read_b64_tr_b16 a[36:37], v102 offset:2048\n"
        "ds_read_b64_tr_b16 a[38:39], v102 offset:18432\n"
        "ds_read_b64_tr_b16 a[96:97], v103 offset:0\n"
        "ds_read_b64_tr_b16 a[98:99], v103 offset:16384\n"
        "ds_read_b64_tr_b16 a[100:101], v103 offset:2048\n"
        "ds_read_b64_tr_b16 a[102:103], v103 offset:18432\n"
        "ds_read_b64_tr_b16 a[40:41], v102 offset:4096\n"
        "ds_read_b64_tr_b16 a[42:43], v102 offset:20480\n"
        "ds_read_b64_tr_b16 a[44:45], v102 offset:6144\n"
        "ds_read_b64_tr_b16 a[46:47], v102 offset:22528\n"
        "ds_read_b64_tr_b16 a[104:105], v103 offset:4096\n"
        "ds_read_b64_tr_b16 a[106:107], v103 offset:20480\n"
        "ds_read_b64_tr_b16 a[108:109], v103 offset:6144\n"
        "ds_read_b64_tr_b16 a[110:111], v103 offset:22528\n"
        "s_waitcnt lgkmcnt(0)\n"
        "s_barrier\n"
        "s_add_u32 m0, s54, 131072\n"
        "s_add_u32 s53, s52, 256\n"
        "buffer_load_dwordx4 v106, s[68:71], s53 offen lds\n"
        "s_add_u32 m0, s54, 132096\n"
        "s_add_u32 s53, s52, 8448\n"
        "buffer_load_dwordx4 v106, s[68:71], s53 offen lds\n"
        "s_add_u32 m0, s54, 133120\n"
        "s_add_u32 s53, s52, 33024\n"
        "buffer_load_dwordx4 v106, s[68:71], s53 offen lds\n"
        "s_add_u32 m0, s54, 134144\n"
        "s_add_u32 s53, s52, 41216\n"
        "buffer_load_dwordx4 v106, s[68:71], s53 offen lds\n"
        "s_add_u32 m0, s54, 135168\n"
        "s_add_u32 s53, s52, 65792\n"
        "buffer_load_dwordx4 v106, s[68:71], s53 offen lds\n"
        "s_add_u32 m0, s54, 136192\n"
        "s_add_u32 s53, s52, 73984\n"
        "buffer_load_dwordx4 v106, s[68:71], s53 offen lds\n"
        "s_add_u32 m0, s54, 137216\n"
        "s_add_u32 s53, s52, 98560\n"
        "buffer_load_dwordx4 v106, s[68:71], s53 offen lds\n"
        "s_add_u32 m0, s54, 138240\n"
        "s_add_u32 s53, s52, 106752\n"
        "buffer_load_dwordx4 v106, s[68:71], s53 offen lds\n"
        "s_waitcnt vmcnt(32)\n"
        "s_barrier\n"
        "ds_read_b64_tr_b16 a[48:49], v110 offset:0\n"
        "ds_read_b64_tr_b16 a[50:51], v110 offset:16384\n"
        "ds_read_b64_tr_b16 a[52:53], v110 offset:2048\n"
        "ds_read_b64_tr_b16 a[54:55], v110 offset:18432\n"
        "ds_read_b64_tr_b16 a[112:113], v111 offset:0\n"
        "ds_read_b64_tr_b16 a[114:115], v111 offset:16384\n"
        "ds_read_b64_tr_b16 a[116:117], v111 offset:2048\n"
        "ds_read_b64_tr_b16 a[118:119], v111 offset:18432\n"
        "ds_read_b64_tr_b16 a[56:57], v110 offset:4096\n"
        "ds_read_b64_tr_b16 a[58:59], v110 offset:20480\n"
        "ds_read_b64_tr_b16 a[60:61], v110 offset:6144\n"
        "ds_read_b64_tr_b16 a[62:63], v110 offset:22528\n"
        "ds_read_b64_tr_b16 a[120:121], v111 offset:4096\n"
        "ds_read_b64_tr_b16 a[122:123], v111 offset:20480\n"
        "ds_read_b64_tr_b16 a[124:125], v111 offset:6144\n"
        "ds_read_b64_tr_b16 a[126:127], v111 offset:22528\n"
        "s_waitcnt lgkmcnt(0)\n"
        "s_barrier\n"
        "s_waitcnt vmcnt(24)\n"
        "s_barrier\n"
        "ds_read_b64_tr_b16 a[128:129], v110 offset:32768\n"
        "ds_read_b64_tr_b16 a[130:131], v110 offset:49152\n"
        "ds_read_b64_tr_b16 a[132:133], v110 offset:34816\n"
        "ds_read_b64_tr_b16 a[134:135], v110 offset:51200\n"
        "ds_read_b64_tr_b16 a[192:193], v111 offset:32768\n"
        "ds_read_b64_tr_b16 a[194:195], v111 offset:49152\n"
        "ds_read_b64_tr_b16 a[196:197], v111 offset:34816\n"
        "ds_read_b64_tr_b16 a[198:199], v111 offset:51200\n"
        "ds_read_b64_tr_b16 a[136:137], v110 offset:36864\n"
        "ds_read_b64_tr_b16 a[138:139], v110 offset:53248\n"
        "ds_read_b64_tr_b16 a[140:141], v110 offset:38912\n"
        "ds_read_b64_tr_b16 a[142:143], v110 offset:55296\n"
        "ds_read_b64_tr_b16 a[200:201], v111 offset:36864\n"
        "ds_read_b64_tr_b16 a[202:203], v111 offset:53248\n"
        "ds_read_b64_tr_b16 a[204:205], v111 offset:38912\n"
        "ds_read_b64_tr_b16 a[206:207], v111 offset:55296\n"
        "s_waitcnt lgkmcnt(0)\n"
        "s_barrier\n"
        "s_waitcnt vmcnt(16)\n"
        "s_barrier\n"
        "ds_write_b128 v119, v[120:123] offset:0\n"
        "ds_write_b128 v119, v[124:127] offset:4096\n"
        "ds_write_b128 v0, v[92:95] offset:16384\n"
        "ds_write_b128 v0, v[96:99] offset:17408\n"
        "v_readfirstlane_b32 s47, v19\n"
        "s_add_u32 s47, s47, 1\n"
        "buffer_load_dwordx2 v[16:17], v12, s[40:43], s77 offen sc1\n"
        "ds_read_b64_tr_b16 a[144:145], v112 offset:0\n"
        "ds_read_b64_tr_b16 a[146:147], v112 offset:16384\n"
        "ds_read_b64_tr_b16 a[148:149], v112 offset:2048\n"
        "ds_read_b64_tr_b16 a[150:151], v112 offset:18432\n"
        "ds_read_b64_tr_b16 a[208:209], v113 offset:0\n"
        "ds_read_b64_tr_b16 a[210:211], v113 offset:16384\n"
        "ds_read_b64_tr_b16 a[212:213], v113 offset:2048\n"
        "ds_read_b64_tr_b16 a[214:215], v113 offset:18432\n"
        "ds_read_b64_tr_b16 a[152:153], v112 offset:4096\n"
        "ds_read_b64_tr_b16 a[154:155], v112 offset:20480\n"
        "ds_read_b64_tr_b16 a[156:157], v112 offset:6144\n"
        "ds_read_b64_tr_b16 a[158:159], v112 offset:22528\n"
        "ds_read_b64_tr_b16 a[216:217], v113 offset:4096\n"
        "ds_read_b64_tr_b16 a[218:219], v113 offset:20480\n"
        "ds_read_b64_tr_b16 a[220:221], v113 offset:6144\n"
        "ds_read_b64_tr_b16 a[222:223], v113 offset:22528\n"
        "s_waitcnt lgkmcnt(0)\n"
        "s_barrier\n"
        "s_waitcnt vmcnt(9)\n"
        "s_barrier\n"
        "ds_read_b64_tr_b16 v[24:25], v112 offset:32768\n"
        "ds_read_b64_tr_b16 v[26:27], v112 offset:49152\n"
        "ds_read_b64_tr_b16 v[28:29], v112 offset:34816\n"
        "ds_read_b64_tr_b16 v[30:31], v112 offset:51200\n"
        "ds_read_b64_tr_b16 v[40:41], v113 offset:32768\n"
        "ds_read_b64_tr_b16 v[42:43], v113 offset:49152\n"
        "ds_read_b64_tr_b16 v[44:45], v113 offset:34816\n"
        "ds_read_b64_tr_b16 v[46:47], v113 offset:51200\n"
        "ds_read_b64_tr_b16 v[32:33], v112 offset:36864\n"
        "ds_read_b64_tr_b16 v[34:35], v112 offset:53248\n"
        "ds_read_b64_tr_b16 v[36:37], v112 offset:38912\n"
        "ds_read_b64_tr_b16 v[38:39], v112 offset:55296\n"
        "ds_read_b64_tr_b16 v[48:49], v113 offset:36864\n"
        "ds_read_b64_tr_b16 v[50:51], v113 offset:53248\n"
        "ds_read_b64_tr_b16 v[52:53], v113 offset:38912\n"
        "ds_read_b64_tr_b16 v[54:55], v113 offset:55296\n"
        "s_waitcnt lgkmcnt(0)\n"
        "s_barrier\n"
        "ds_write_b128 v2, v[24:27] offset:0\n"
        "ds_write_b128 v2, v[28:31] offset:1024\n"
        "ds_write_b128 v2, v[32:35] offset:2048\n"
        "ds_write_b128 v2, v[36:39] offset:3072\n"
        "ds_write_b128 v2, v[40:43] offset:4096\n"
        "ds_write_b128 v2, v[44:47] offset:5120\n"
        "ds_write_b128 v2, v[48:51] offset:6144\n"
        "ds_write_b128 v2, v[52:55] offset:7168\n"
        "s_waitcnt lgkmcnt(0)\n"
        "s_waitcnt vmcnt(0)\n"
        "s_barrier\n"
        "ds_read_b64_tr_b16 v[24:25], v102 offset:0\n"
        "ds_read_b64_tr_b16 v[26:27], v102 offset:16384\n"
        "ds_read_b64_tr_b16 v[28:29], v102 offset:2048\n"
        "ds_read_b64_tr_b16 v[30:31], v102 offset:18432\n"
        "ds_read_b64_tr_b16 v[40:41], v103 offset:0\n"
        "ds_read_b64_tr_b16 v[42:43], v103 offset:16384\n"
        "ds_read_b64_tr_b16 v[44:45], v103 offset:2048\n"
        "ds_read_b64_tr_b16 v[46:47], v103 offset:18432\n"
        "ds_read_b64_tr_b16 v[32:33], v102 offset:4096\n"
        "ds_read_b64_tr_b16 v[34:35], v102 offset:20480\n"
        "ds_read_b64_tr_b16 v[36:37], v102 offset:6144\n"
        "ds_read_b64_tr_b16 v[38:39], v102 offset:22528\n"
        "ds_read_b64_tr_b16 v[48:49], v103 offset:4096\n"
        "ds_read_b64_tr_b16 v[50:51], v103 offset:20480\n"
        "ds_read_b64_tr_b16 v[52:53], v103 offset:6144\n"
        "ds_read_b64_tr_b16 v[54:55], v103 offset:22528\n"
        "s_waitcnt lgkmcnt(0)\n"
        "s_barrier\n"
        "ds_write_b128 v2, v[24:27] offset:32768\n"
        "ds_write_b128 v2, v[28:31] offset:33792\n"
        "ds_write_b128 v2, v[32:35] offset:34816\n"
        "ds_write_b128 v2, v[36:39] offset:35840\n"
        "ds_write_b128 v2, v[40:43] offset:36864\n"
        "ds_write_b128 v2, v[44:47] offset:37888\n"
        "ds_write_b128 v2, v[48:51] offset:38912\n"
        "ds_write_b128 v2, v[52:55] offset:39936\n"
        "s_waitcnt lgkmcnt(0)\n"
        "LH_G2:\n"
        "s_lshl_b32 s58, s44, 19\n"
        "s_lshl_b32 s59, s45, 11\n"
        "s_add_u32 s58, s58, s59\n"
        "s_add_u32 s52, s58, 75497472\n"
        "s_add_u32 s53, s58, 92274688\n"
        "s_lshl_b32 s58, s44, 17\n"
        "s_lshl_b32 s59, s45, 9\n"
        "s_add_u32 s58, s58, s59\n"
        "s_add_u32 s54, s58, 39845888\n"
        "s_lshl_b32 s58, s44, 18\n"
        "s_add_u32 s55, s58, 44040192\n"
        "s_lshl_b32 s74, s45, 10\n"
        "s_add_u32 s74, s74, 32768\n"
        "s_mov_b32 s46, 0\n"
        "s_mov_b32 s48, 0\n"
        "s_mov_b32 s51, 0\n"
        "s_lshl_b32 s75, s79, 13\n"
        "s_add_u32 s75, s75, 65536\n"
        "s_cmp_eq_u32 s45, 0\n"
        "s_cselect_b32 s75, s75, 2146435072\n"
        "s_cmp_eq_u32 s79, 0\n"
        "s_cselect_b32 s68, s54, 2146435072\n"
        "s_cselect_b32 s69, 2146435072, s55\n"
        "s_cselect_b32 s70, 2048, 0\n"
        "s_cselect_b32 s71, 0, 4096\n"
        "s_mov_b32 s54, 2146435072\n"
        "s_mov_b32 s55, 2146435072\n"
        "v_cmp_eq_u32 vcc, s47, v17\n"
        "s_nop 1\n"
        "v_cndmask_b32 v16, 0, v16, vcc\n"
        "s_nop 0\n"
        "v_readlane_b32 s48, v16, 0\n"
        "v_readlane_b32 s58, v16, 1\n"
        "s_min_u32 s48, s48, s58\n"
        "s_mov_b32 s59, 1\n"
        "s_cmp_ge_u32 s48, s59\n"
        "s_cbranch_scc1 LH_P0_OK\n"
        "s_mov_b32 s57, 0\n"
        "LH_P0_P:\n"
        "buffer_load_dwordx2 v[16:17], v12, s[40:43], s77 offen sc1\n"
        "s_waitcnt vmcnt(0)\n"
        "v_cmp_eq_u32 vcc, s47, v17\n"
        "s_nop 1\n"
        "v_cndmask_b32 v16, 0, v16, vcc\n"
        "s_nop 0\n"
        "v_readlane_b32 s48, v16, 0\n"
        "v_readlane_b32 s58, v16, 1\n"
        "s_min_u32 s48, s48, s58\n"
        "s_cmp_ge_u32 s48, s59\n"
        "s_cbranch_scc1 LH_P0_OK\n"
        "s_sleep 2\n"
        "s_add_u32 s57, s57, 1\n"
        "s_cmp_lt_u32 s57, 400000\n"
        "s_cbranch_scc1 LH_P0_P\n"
        "s_branch LH_ABORT\n"
        "LH_P0_OK:\n"
        "s_mov_b32 m0, s74\n"
        "s_nop 0\n"
        "buffer_load_dwordx4 v0, s[40:43], s52 offen sc1 lds\n"
        "s_add_u32 m0, s74, 3072\n"
        "s_nop 0\n"
        "buffer_load_dwordx4 v0, s[40:43], s52 offen offset:1024 sc1 lds\n"
        "s_add_u32 m0, s74, 8192\n"
        "s_nop 0\n"
        "buffer_load_dwordx4 v0, s[40:43], s53 offen sc1 lds\n"
        "s_add_u32 s52, s52, 8192\n"
        "s_add_u32 m0, s74, 11264\n"
        "s_nop 0\n"
        "buffer_load_dwordx4 v0, s[40:43], s53 offen offset:1024 sc1 lds\n"
        "s_add_u32 s53, s53, 8192\n"
        "s_mov_b32 s84, s52\n"
        "s_mov_b32 s85, s53\n"
        "s_mov_b32 s52, 2146435072\n"
        "s_mov_b32 s53, 2146435072\n"
        "v_lshlrev_b32 v118, 3, v104\n"
        "s_mov_b32 s57, 0\n"
        "LH_RDY:\n"
        "buffer_load_dwordx2 v[120:121], v118, s[40:43], 0 offen offset:1024 sc1\n"
        "s_waitcnt vmcnt(0)\n"
        "v_cmp_ne_u32 vcc, s47, v121\n"
        "s_cbranch_vccz LH_RDYOK\n"
        "s_sleep 4\n"
        "s_add_u32 s57, s57, 1\n"
        "s_cmp_lt_u32 s57, 400000\n"
        "s_cbranch_scc1 LH_RDY\n"
        "s_branch LH_ABORT\n"
        "LH_RDYOK:\n"
        "v_mov_b32_dpp v121, v120 row_ror:1 row_mask:0xf bank_mask:0xf\n"
        "v_pk_min_u16 v120, v120, v121\n"
        "s_nop 1\n"
        "v_mov_b32_dpp v121, v120 row_ror:2 row_mask:0xf bank_mask:0xf\n"
        "v_pk_min_u16 v120, v120, v121\n"
        "s_nop 1\n"
        "v_mov_b32_dpp v121, v120 row_ror:4 row_mask:0xf bank_mask:0xf\n"
        "v_pk_min_u16 v120, v120, v121\n"
        "s_nop 1\n"
        "v_mov_b32_dpp v121, v120 row_ror:8 row_mask:0xf bank_mask:0xf\n"
        "v_pk_min_u16 v120, v120, v121\n"
        "v_mov_b32 v121, v120\n"
        "s_nop 1\n"
        "v_permlane16_swap_b32 v120, v121\n"
        "v_pk_min_u16 v120, v120, v121\n"
        "v_mov_b32 v121, v120\n"
        "s_nop 1\n"
        "v_permlane32_swap_b32 v120, v121\n"
        "v_pk_min_u16 v120, v120, v121\n"
        "s_nop 0\n"
        "v_readfirstlane_b32 s58, v120\n"
        "s_lshr_b32 s59, s58, 16\n"
        "s_and_b32 s58, s58, 0xffff\n"
        "s_cmp_lt_u32 s59, s58\n"
        "s_cselect_b32 s73, 8192, 0\n"
        "s_add_u32 s56, s73, 3145728\n"
        "v_lshrrev_b32 v114, 3, v104\n"
        "s_lshl_b32 s58, s79, 2\n"
        "s_add_u32 s58, s58, s45\n"
        "s_lshl_b32 s58, s58, 17\n"
        "s_lshl_b32 s59, s44, 7\n"
        "s_add_u32 s58, s58, s59\n"
        "s_add_u32 s58, s58, s73\n"
        "s_add_u32 s58, s58, 2097152\n"
        "v_and_b32 v115, 7, v104\n"
        "v_lshlrev_b32 v115, 4, v115\n"
        "v_lshl_add_u32 v4, v114, 14, v115\n"
        "v_add_u32 v4, s58, v4\n"
        "v_mov_b64 v[56:57], 0\n"
        "v_mov_b64 v[58:59], 0\n"
        "v_mov_b64 v[60:61], 0\n"
        "v_mov_b64 v[62:63], 0\n"
        "v_mov_b64 v[64:65], 0\n"
        "v_mov_b64 v[66:67], 0\n"
        "v_mov_b64 v[68:69], 0\n"
        "v_mov_b64 v[70:71], 0\n"
        "v_mov_b64 v[72:73], 0\n"
        "v_mov_b64 v[74:75], 0\n"
        "v_mov_b64 v[76:77], 0\n"
        "v_mov_b64 v[78:79], 0\n"
        "v_mov_b64 v[20:21], 0\n"
        "v_mov_b64 v[22:23], 0\n"
        "v_mov_b64 v[18:19], 0\n"
        "ds_read_b128 v[112:115], v0 offset:16384\n"
        "ds_read_b128 v[116:119], v0 offset:17408\n"
        "s_not_b64 vcc, s[62:63]\n"
        "s_waitcnt vmcnt(0) lgkmcnt(0)\n"
        "s_barrier\n"
        "s_memrealtime s[24:25]\n"
        "s_memtime s[26:27]\n"
        "s_or_b32 s58, s79, s45\n"
        "s_lshr_b32 s59, s44, 2\n"
        "s_or_b32 s58, s58, s59\n"
        "s_lshl_b32 s28, s44, 3\n"
        "s_add_u32 s28, s28, s56\n"
        "s_add_u32 s28, s28, 81920\n"
        "s_cmp_eq_u32 s58, 0\n"
        "s_cselect_b32 s28, s28, 2146435072\n"
        "s_waitcnt lgkmcnt(0)\n"
        "s_branch LH_STEP\n"
        "LH_TOP:\n"
        "s_cmp_eq_u32 s46, 63\n"
        "s_cbranch_scc1 LH_FINB\n"
        "s_not_b64 vcc, s[62:63]\n"
        "s_waitcnt lgkmcnt(0)\n"
        "s_barrier\n"
        "LH_STEP:\n"
        "ds_read_b128 v[24:27], v13 offset:0\n"
        "ds_read_b128 v[28:31], v13 offset:2048\n"
        "ds_read_b128 v[32:35], v14 offset:0\n"
        "s_waitcnt lgkmcnt(2)\n"
        "v_smfmac_f32_16x16x64_bf16 v[56:59], v[24:27], a[0:7], v3\n"
        "ds_read_b128 v[36:39], v14 offset:2048\n"
        "v_smfmac_f32_16x16x64_bf16 v[60:63], v[24:27], a[64:71], v3\n"
        "ds_read_b128 v[40:43], v13 offset:4096\n"
        "v_smfmac_f32_16x16x64_bf16 v[64:67], v[24:27], a[128:135], v3\n"
        "ds_read_b128 v[44:47], v13 offset:6144\n"
        "v_smfmac_f32_16x16x64_bf16 v[68:71], v[24:27], a[192:199], v3\n"
        "ds_read_b128 v[48:51], v14 offset:4096\n"
        "s_waitcnt lgkmcnt(5)\n"
        "v_smfmac_f32_16x16x64_bf16 v[56:59], v[28:31], a[8:15], v3\n"
        "ds_read_b128 v[52:55], v14 offset:6144\n"
        "v_smfmac_f32_16x16x64_bf16 v[60:63], v[28:31], a[72:79], v3\n"
        "ds_read_b128 v[80:83], v1 offset:32768\n"
        "v_smfmac_f32_16x16x64_bf16 v[64:67], v[28:31], a[136:143], v3\n"
        "ds_read_b128 v[84:87], v1 offset:36864\n"
        "v_smfmac_f32_16x16x64_bf16 v[68:71], v[28:31], a[200:207], v3\n"
        "ds_read_b64 v[88:89], v10 offset:40960\n"
        "s_waitcnt lgkmcnt(8)\n"
        "v_smfmac_f32_16x16x64_bf16 v[56:59], v[32:35], a[16:23], v3\n"
        "ds_read_b64 v[92:93], v10 offset:41472\n"
        "v_smfmac_f32_16x16x64_bf16 v[60:63], v[32:35], a[80:87], v3\n"
        "ds_read_b64 v[96:97], v10 offset:45056\n"
        "v_smfmac_f32_16x16x64_bf16 v[64:67], v[32:35], a[144:151], v3\n"
        "ds_read_b64 v[100:101], v10 offset:45568\n"
        "v_smfmac_f32_16x16x64_bf16 v[68:71], v[32:35], a[208:215], v3\n"
        "s_waitcnt lgkmcnt(10)\n"
        "v_smfmac_f32_16x16x64_bf16 v[56:59], v[36:39], a[24:31], v3\n"
        "v_smfmac_f32_16x16x64_bf16 v[60:63], v[36:39], a[88:95], v3\n"
        "v_smfmac_f32_16x16x64_bf16 v[64:67], v[36:39], a[152:159], v3\n"
        "v_smfmac_f32_16x16x64_bf16 v[68:71], v[36:39], a[216:223], v3\n"
        "s_waitcnt lgkmcnt(9)\n"
        "v_smfmac_f32_16x16x64_bf16 v[56:59], v[40:43], a[32:39], v3\n"
        "v_smfmac_f32_16x16x64_bf16 v[60:63], v[40:43], a[96:103], v3\n"
        "v_smfmac_f32_16x16x64_bf16 v[64:67], v[40:43], a[160:167], v3\n"
        "v_smfmac_f32_16x16x64_bf16 v[68:71], v[40:43], a[224:231], v3\n"
        "s_mov_b32 m0, s74\n"
        "s_waitcnt lgkmcnt(0)\n"
        "v_smfmac_f32_16x16x64_bf16 v[56:59], v[44:47], a[40:47], v3\n"
        "buffer_load_dwordx4 v0, s[40:43], s52 offen sc1 lds\n"
        "v_smfmac_f32_16x16x64_bf16 v[60:63], v[44:47], a[104:111], v3\n"
        "v_smfmac_f32_16x16x64_bf16 v[64:67], v[44:47], a[168:175], v3\n"
        "s_add_u32 m0, s74, 3072\n"
        "v_smfmac_f32_16x16x64_bf16 v[68:71], v[44:47], a[232:239], v3\n"
        "buffer_load_dwordx4 v0, s[40:43], s52 offen offset:1024 sc1 lds\n"
        "v_smfmac_f32_16x16x64_bf16 v[56:59], v[48:51], a[48:55], v3\n"
        "v_smfmac_f32_16x16x64_bf16 v[60:63], v[48:51], a[112:119], v3\n"
        "s_add_u32 m0, s74, 8192\n"
        "v_smfmac_f32_16x16x64_bf16 v[56:59], v[52:55], a[56:63], v3\n"
        "buffer_load_dwordx4 v0, s[40:43], s53 offen sc1 lds\n"
        "v_smfmac_f32_16x16x64_bf16 v[60:63], v[52:55], a[120:127], v3\n"
        "v_smfmac_f32_16x16x64_bf16 v[64:67], v[48:51], a[176:183], v3\n"
        "s_add_u32 m0, s74, 11264\n"
        "v_smfmac_f32_16x16x64_bf16 v[68:71], v[48:51], a[240:247], v3\n"
        "buffer_load_dwordx4 v0, s[40:43], s53 offen offset:1024 sc1 lds\n"
        "v_smfmac_f32_16x16x64_bf16 v[64:67], v[52:55], a[184:191], v3\n"
        "v_add_f32 v56, v56, v57\n"
        "s_add_u32 s52, s52, 8192\n"
        "v_smfmac_f32_16x16x64_bf16 v[68:71], v[52:55], a[248:255], v3\n"
        "v_add_f32 v57, v58, v59\n"
        "s_add_u32 s53, s53, 8192\n"
        "v_smfmac_f32_16x16x64_bf16 v[72:75], v[24:27], v[128:135], v3\n"
        "v_add_f32 v60, v60, v61\n"
        "v_add_f32 v61, v62, v63\n"
        "s_waitcnt vmcnt(4)\n"
        "v_cvt_pk_bf16_f32 v104, v56, v57\n"
        "v_add_f32 v64, v64, v65\n"
        "v_add_f32 v65, v66, v67\n"
        "v_cvt_pk_bf16_f32 v105, v60, v61\n"
        "v_add_f32 v68, v68, v69\n"
        "v_add_f32 v69, v70, v71\n"
        "v_cvt_pk_bf16_f32 v106, v64, v65\n"
        "v_cvt_pk_bf16_f32 v107, v68, v69\n"
        "v_smfmac_f32_16x16x64_bf16 v[76:79], v[24:27], v[192:199], v3\n"
        "v_mfma_f32_16x16x32_bf16 v[56:59], v[104:107], v[112:115], 0\n"
        "v_mfma_f32_16x16x32_bf16 v[64:67], v[104:107], v[116:119], 0\n"
        "v_smfmac_f32_16x16x64_bf16 v[72:75], v[28:31], v[136:143], v3\n"
        "v_cvt_pk_bf16_f32 v20, v56, v57\n"
        "v_cvt_pk_bf16_f32 v21, v58, v59\n"
        "v_cvt_pk_bf16_f32 v22, v64, v65\n"
        "v_cvt_pk_bf16_f32 v23, v66, v67\n"
        "v_smfmac_f32_16x16x64_bf16 v[76:79], v[28:31], v[200:207], v3\n"
        "v_mfma_f32_16x16x32_bf16 v[56:59], v[20:23], v[20:23], 0\n"
        "v_mfma_f32_16x16x32_bf16 v[60:63], v[84:87], v[20:23], 0\n"
        "v_mov_b64 v[124:125], 0\n"
        "v_mfma_f32_16x16x32_bf16 v[68:71], v[80:83], v[20:23], 0\n"
        "v_cndmask_b32_dpp v56, v59, v57, vcc quad_perm:[0,0,2,2] row_mask:0xf bank_mask:0xf\n"
        "ds_write_b32 v6, v56 offset:12800\n"
        "v_smfmac_f32_16x16x64_bf16 v[72:75], v[32:35], v[144:151], v3\n"
        "v_mov_b64 v[126:127], 0\n"
        "s_nop 0\n"
        "v_cndmask_b32_dpp v68, v60, v68, vcc quad_perm:[0,0,2,2] row_mask:0xf bank_mask:0xf\n"
        "v_cndmask_b32_dpp v69, v61, v69, vcc quad_perm:[0,0,2,2] row_mask:0xf bank_mask:0xf\n"
        "v_cndmask_b32_dpp v70, v62, v70, vcc quad_perm:[0,0,2,2] row_mask:0xf bank_mask:0xf\n"
        "v_cndmask_b32_dpp v71, v63, v71, vcc quad_perm:[0,0,2,2] row_mask:0xf bank_mask:0xf\n"
        "ds_write_b128 v1, v[68:71] offset:8704\n"
        "v_smfmac_f32_16x16x64_bf16 v[76:79], v[32:35], v[208:215], v3\n"
        "v_mov_b64 v[80:81], 0\n"
        "v_smfmac_f32_16x16x64_bf16 v[72:75], v[36:39], v[152:159], v3\n"
        "v_mov_b64 v[82:83], 0\n"
        "v_smfmac_f32_16x16x64_bf16 v[76:79], v[36:39], v[216:223], v3\n"
        "v_mov_b64 v[84:85], 0\n"
        "v_mov_b64 v[86:87], 0\n"
        "s_waitcnt lgkmcnt(0)\n"
        "s_barrier\n"
        "ds_read_b128 v[104:107], v0 offset:8704\n"
        "ds_read_b32 v120, v5 offset:12800\n"
        "ds_read_b128 v[108:111], v0 offset:9728\n"
        "ds_read_b32 v121, v5 offset:13056\n"
        "ds_read_b128 v[112:115], v0 offset:10752\n"
        "ds_read_b32 v122, v5 offset:13312\n"
        "ds_read_b128 v[116:119], v0 offset:11776\n"
        "ds_read_b32 v123, v5 offset:13568\n"
        "v_smfmac_f32_16x16x64_bf16 v[72:75], v[40:43], v[160:167], v3\n"
        "ds_read_b128 v[24:27], v2 offset:0\n"
        "v_smfmac_f32_16x16x64_bf16 v[76:79], v[40:43], v[224:231], v3\n"
        "ds_read_b128 v[28:31], v2 offset:1024\n"
        "v_smfmac_f32_16x16x64_bf16 v[72:75], v[44:47], v[168:175], v3\n"
        "ds_read_b128 v[56:59], v2 offset:32768\n"
        "v_smfmac_f32_16x16x64_bf16 v[76:79], v[44:47], v[232:239], v3\n"
        "ds_read_b128 v[60:63], v2 offset:33792\n"
        "v_smfmac_f32_16x16x64_bf16 v[72:75], v[48:51], v[176:183], v3\n"
        "ds_read_b128 v[64:67], v2 offset:34816\n"
        "v_smfmac_f32_16x16x64_bf16 v[76:79], v[48:51], v[240:247], v3\n"
        "ds_read_b128 v[68:71], v2 offset:35840\n"
        "s_waitcnt lgkmcnt(10)\n"
        "v_add_f32 v104, v104, v108\n"
        "v_add_f32 v105, v105, v109\n"
        "v_add_f32 v106, v106, v110\n"
        "v_add_f32 v107, v107, v111\n"
        "v_add_f32 v120, v120, v121\n"
        "s_waitcnt lgkmcnt(6)\n"
        "v_add_f32 v112, v112, v116\n"
        "v_add_f32 v113, v113, v117\n"
        "v_add_f32 v114, v114, v118\n"
        "v_add_f32 v115, v115, v119\n"
        "v_add_f32 v122, v122, v123\n"
        "v_add_f32 v104, v104, v112\n"
        "v_add_f32 v105, v105, v113\n"
        "v_add_f32 v106, v106, v114\n"
        "v_add_f32 v107, v107, v115\n"
        "v_add_f32 v120, v120, v122\n"
        "v_max3_f32 v108, v104, v105, v106\n"
        "v_max3_f32 v108, v108, v107, v120\n"
        "v_smfmac_f32_16x16x64_bf16 v[72:75], v[52:55], v[184:191], v3\n"
        "ds_read_b128 v[32:35], v2 offset:2048\n"
        "v_max_f32_dpp v108, v108, v108 quad_perm:[1,0,3,2] row_mask:0xf bank_mask:0xf\n"
        "v_mov_b32 v109, v108\n"
        "v_smfmac_f32_16x16x64_bf16 v[76:79], v[52:55], v[248:255], v3\n"
        "ds_read_b128 v[36:39], v2 offset:3072\n"
        "v_permlane16_swap_b32 v108, v109\n"
        "v_max_f32 v108, v108, v109\n"
        "v_mov_b32 v109, v108\n"
        "v_mov_b64 v[112:113], s[60:61]\n"
        "v_mov_b64 v[114:115], s[60:61]\n"
        "v_permlane32_swap_b32 v108, v109\n"
        "v_max_f32 v108, v108, v109\n"
        "v_mul_f32 v108, s76, v108\n"
        "v_fma_f32 v120, v120, s72, v108\n"
        "v_fma_f32 v104, v104, s72, v108\n"
        "v_fma_f32 v105, v105, s72, v108\n"
        "v_fma_f32 v106, v106, s72, v108\n"
        "v_fma_f32 v107, v107, s72, v108\n"
        "v_exp_f32 v120, v120\n"
        "v_exp_f32 v104, v104\n"
        "v_exp_f32 v105, v105\n"
        "v_exp_f32 v106, v106\n"
        "v_exp_f32 v107, v107\n"
        "v_cvt_pk_bf16_f32 v121, v120, 0\n"
        "v_cvt_pk_bf16_f32 v108, v104, v105\n"
        "v_cndmask_b32 v109, 0, v121, s[66:67]\n"
        "v_cvt_pk_bf16_f32 v110, v106, v107\n"
        "v_cndmask_b32 v111, v121, 0, s[66:67]\n"
        "v_mov_b64 v[116:117], s[60:61]\n"
        "v_mov_b64 v[118:119], s[60:61]\n"
        "v_add_f32 v72, v72, v73\n"
        "v_add_f32 v73, v74, v75\n"
        "v_smfmac_f32_16x16x64_bf16 v[84:87], v[108:111], v[112:119], v3\n"
        "ds_read_b128 v[40:43], v2 offset:4096\n"
        "v_add_f32 v76, v76, v77\n"
        "v_add_f32 v77, v78, v79\n"
        "v_cvt_pk_bf16_f32 v90, v72, 0\n"
        "v_cvt_pk_bf16_f32 v91, v73, 0\n"
        "v_mov_b64 v[94:95], v[90:91]\n"
        "v_cvt_pk_bf16_f32 v98, v76, 0\n"
        "v_cvt_pk_bf16_f32 v99, v77, 0\n"
        "v_smfmac_f32_16x16x64_bf16 v[124:127], v[108:111], v[88:95], v3\n"
        "ds_read_b128 v[44:47], v2 offset:5120\n"
        "v_mov_b64 v[102:103], v[98:99]\n"
        "v_add_f32 v84, v84, v85\n"
        "v_add_f32 v85, v86, v87\n"
        "v_smfmac_f32_16x16x64_bf16 v[80:83], v[108:111], v[96:103], v3\n"
        "ds_read_b128 v[48:51], v2 offset:6144\n"
        "v_rcp_f32 v84, v84\n"
        "v_rcp_f32 v85, v85\n"
        "v_add_f32 v124, v124, v125\n"
        "v_add_f32 v125, v126, v127\n"
        "v_mul_f32 v124, v124, v84\n"
        "v_mul_f32 v125, v125, v85\n"
        "v_add_f32 v80, v80, v81\n"
        "v_add_f32 v81, v82, v83\n"
        "v_mul_f32 v80, v80, v84\n"
        "v_mul_f32 v81, v81, v85\n"
        "v_cvt_pk_bf16_f32 v126, v124, v80\n"
        "v_cvt_pk_bf16_f32 v127, v125, v81\n"
        "ds_write_b64 v7, v[126:127]\n"
        "v_cvt_pk_bf16_f32 v18, v72, v73\n"
        "v_cvt_pk_bf16_f32 v19, v76, v77\n"
        "ds_read_b128 v[52:55], v2 offset:7168\n"
        "ds_read_b128 v[72:75], v2 offset:36864\n"
        "ds_read_b128 v[76:79], v2 offset:37888\n"
        "v_mov_b64 v[88:89], 0\n"
        "v_mov_b64 v[90:91], 0\n"
        "v_mov_b64 v[92:93], 0\n"
        "v_mov_b64 v[94:95], 0\n"
        "v_mov_b64 v[96:97], 0\n"
        "v_mov_b64 v[98:99], 0\n"
        "v_mov_b64 v[100:101], 0\n"
        "v_mov_b64 v[102:103], 0\n"
        "s_waitcnt lgkmcnt(3)\n"
        "s_barrier\n"
        "ds_read_b64_tr_b16 v[104:105], v8\n"
        "ds_read_b64_tr_b16 v[106:107], v9\n"
        "ds_read_b64_tr_b16 v[108:109], v8 offset:1024\n"
        "ds_read_b64_tr_b16 v[110:111], v9 offset:1024\n"
        "ds_read_b128 v[80:83], v2 offset:38912\n"
        "ds_read_b128 v[84:87], v2 offset:39936\n"
        "s_waitcnt lgkmcnt(4)\n"
        "v_smfmac_f32_16x16x64_bf16 v[88:91], v[104:107], v[24:31], v3\n"
        "v_smfmac_f32_16x16x64_bf16 v[92:95], v[104:107], v[40:47], v3\n"
        "v_smfmac_f32_16x16x64_bf16 v[96:99], v[104:107], v[56:63], v3\n"
        "s_waitcnt lgkmcnt(2)\n"
        "v_smfmac_f32_16x16x64_bf16 v[88:91], v[108:111], v[32:39], v3\n"
        "v_smfmac_f32_16x16x64_bf16 v[92:95], v[108:111], v[48:55], v3\n"
        "v_smfmac_f32_16x16x64_bf16 v[96:99], v[108:111], v[64:71], v3\n"
        "s_waitcnt lgkmcnt(2)\n"
        "v_smfmac_f32_16x16x64_bf16 v[100:103], v[104:107], v[72:79], v3\n"
        "v_add_f32 v88, v88, v89\n"
        "s_waitcnt lgkmcnt(0)\n"
        "v_smfmac_f32_16x16x64_bf16 v[100:103], v[108:111], v[80:87], v3\n"
        "v_add_f32 v89, v90, v91\n"
        "v_add_f32 v92, v92, v93\n"
        "v_add_f32 v93, v94, v95\n"
        "v_cvt_pk_bf16_f32 v112, v88, v89\n"
        "v_add_f32 v96, v96, v97\n"
        "v_add_f32 v97, v98, v99\n"
        "v_cvt_pk_bf16_f32 v113, v92, v93\n"
        "v_cvt_pk_bf16_f32 v114, v96, v97\n"
        "v_add_f32 v100, v100, v101\n"
        "v_add_f32 v101, v102, v103\n"
        "v_cvt_pk_bf16_f32 v115, v100, v101\n"
        "buffer_store_dwordx4 v[112:115], v4, s[40:43], s51 offen sc1\n"
        "s_mov_b32 s54, s68\n"
        "s_mov_b32 s55, s69\n"
        "s_add_u32 s68, s68, s70\n"
        "s_add_u32 s69, s69, s71\n"
        "s_xor_b32 s51, s51, 4096\n"
        "s_and_b32 s58, s46, 3\n"
        "s_lshl_b32 s58, s58, 14\n"
        "s_add_u32 s49, s58, s56\n"
        "s_add_u32 s50, s49, 4096\n"
        "v_mov_b32 v40, s46\n"
        "v_mov_b32 v41, s47\n"
        "s_add_u32 s46, s46, 1\n"
        "s_add_u32 s59, s46, 2\n"
        "s_min_u32 s59, s59, 64\n"
        "v_cmp_eq_u32 vcc, s47, v17\n"
        "s_nop 1\n"
        "v_cndmask_b32 v16, 0, v16, vcc\n"
        "s_nop 0\n"
        "v_readlane_b32 s48, v16, 0\n"
        "v_readlane_b32 s58, v16, 1\n"
        "s_min_u32 s48, s48, s58\n"
        "s_cmp_ge_u32 s48, s59\n"
        "s_cbranch_scc1 LH_PN_OK\n"
        "s_mov_b32 s57, 0\n"
        "LH_PN_P:\n"
        "buffer_load_dwordx2 v[16:17], v12, s[40:43], s77 offen sc1\n"
        "s_waitcnt vmcnt(0)\n"
        "v_cmp_eq_u32 vcc, s47, v17\n"
        "s_nop 1\n"
        "v_cndmask_b32 v16, 0, v16, vcc\n"
        "s_nop 0\n"
        "v_readlane_b32 s48, v16, 0\n"
        "v_readlane_b32 s58, v16, 1\n"
        "s_min_u32 s48, s48, s58\n"
        "s_cmp_ge_u32 s48, s59\n"
        "s_cbranch_scc1 LH_PN_OK\n"
        "s_sleep 2\n"
        "s_add_u32 s57, s57, 1\n"
        "s_cmp_lt_u32 s57, 400000\n"
        "s_cbranch_scc1 LH_PN_P\n"
        "s_branch LH_ABORT\n"
        "LH_PN_OK:\n"
        "s_cmp_lg_u32 s46, 1\n"
        "s_cbranch_scc1 LH_NF\n"
        "s_mov_b32 s52, s84\n"
        "s_mov_b32 s53, s85\n"
        "s_mov_b32 m0, s74\n"
        "s_nop 0\n"
        "buffer_load_dwordx4 v0, s[40:43], s52 offen sc1 lds\n"
        "s_add_u32 m0, s74, 3072\n"
        "s_nop 0\n"
        "buffer_load_dwordx4 v0, s[40:43], s52 offen offset:1024 sc1 lds\n"
        "s_add_u32 m0, s74, 8192\n"
        "s_nop 0\n"
        "buffer_load_dwordx4 v0, s[40:43], s53 offen sc1 lds\n"
        "s_add_u32 s52, s52, 8192\n"
        "s_add_u32 m0, s74, 11264\n"
        "s_nop 0\n"
        "buffer_load_dwordx4 v0, s[40:43], s53 offen offset:1024 sc1 lds\n"
        "s_add_u32 s53, s53, 8192\n"
        "LH_NF:\n"
        "v_mov_b64 v[56:57], 0\n"
        "v_mov_b64 v[58:59], 0\n"
        "v_mov_b64 v[60:61], 0\n"
        "v_mov_b64 v[62:63], 0\n"
        "v_mov_b64 v[64:65], 0\n"
        "v_mov_b64 v[66:67], 0\n"
        "v_mov_b64 v[68:69], 0\n"
        "v_mov_b64 v[70:71], 0\n"
        "v_mov_b64 v[72:73], 0\n"
        "v_mov_b64 v[74:75], 0\n"
        "v_mov_b64 v[76:77], 0\n"
        "v_mov_b64 v[78:79], 0\n"
        "ds_read_b128 v[112:115], v0 offset:16384\n"
        "ds_read_b128 v[116:119], v0 offset:17408\n"
        "s_sleep 2\n"
        "buffer_store_dwordx4 v[20:23], v11, s[40:43], s54 offen sc1\n"
        "buffer_store_dwordx2 v[18:19], v10, s[40:43], s55 offen sc1\n"
        "s_memrealtime s[96:97]\n"
        "s_memtime s[30:31]\n"
        "v_mov_b32 v45, s47\n"
        "s_waitcnt lgkmcnt(0)\n"
        "s_sub_u32 s96, s96, s24\n"
        "s_sub_u32 s30, s30, s26\n"
        "v_cvt_f32_u32 v44, s96\n"
        "v_cvt_f32_u32 v46, s30\n"
        "v_rcp_f32 v46, v46\n"
        "v_mul_f32 v44, 0x471c4000, v44\n"
        "v_mul_f32 v44, v44, v46\n"
        "v_mov_b32 v46, 0\n"
        "v_cvt_u32_f32 v44, v44\n"
        "buffer_store_dwordx2 v[44:45], v46, s[40:43], s28 offen sc1\n"
        "buffer_store_dwordx2 v[40:41], v12, s[40:43], s75 offen sc1\n"
        "buffer_load_dwordx2 v[16:17], v12, s[40:43], s77 offen sc1\n"
        "s_lshl_b32 s85, s47, 6\n"
        "s_add_u32 s85, s85, s46\n"
        "s_sub_u32 s85, s85, 1\n"
        "s_cmp_eq_u32 s46, 1\n"
        "s_cbranch_scc1 LH_TGO\n"
        "s_add_u32 s84, s56, 65536\n"
        "v_mov_b32 v24, 0\n"
        "s_mov_b32 s57, 0\n"
        "LH_TC:\n"
        "buffer_load_dwordx2 v[26:27], v24, s[40:43], s84 offen sc1\n"
        "s_add_u32 s57, s57, 1\n"
        "s_cmp_gt_u32 s57, 64\n"
        "s_cbranch_scc1 LH_TGO\n"
        "s_waitcnt vmcnt(0)\n"
        "v_readfirstlane_b32 s58, v27\n"
        "v_readfirstlane_b32 s59, v26\n"
        "s_sub_u32 s58, s58, s85\n"
        "s_cmp_eq_u32 s58, 1\n"
        "s_cbranch_scc1 LH_TGO\n"
        "s_cmp_lg_u32 s58, 0\n"
        "s_cbranch_scc1 LH_TC\n"
        "s_add_u32 s59, s59, 44\n"
        "s_and_b32 s84, s59, 1\n"
        "s_memrealtime s[96:97]\n"
        "s_waitcnt lgkmcnt(0)\n"
        "s_lshl_b32 s58, s96, 1\n"
        "s_sub_u32 s58, s59, s58\n"
        "s_ashr_i32 s58, s58, 1\n"
        "s_add_u32 s59, s96, s58\n"
        "s_cmp_gt_u32 s58, 400\n"
        "s_cbranch_scc1 LH_TGO\n"
        "LH_F_W:\n"
        "s_memrealtime s[96:97]\n"
        "s_waitcnt lgkmcnt(0)\n"
        "s_sub_u32 s58, s59, s96\n"
        "s_cmp_gt_i32 s58, 5\n"
        "s_cbranch_scc1 LH_F_W\n"
        "s_max_i32 s58, s58, 0\n"
        "s_lshl_b32 s58, s58, 2\n"
        "s_getpc_b64 s[96:97]\n"
        "LH_F_X:\n"
        "s_sub_u32 s58, LH_F_E-LH_F_X, s58\n"
        "s_add_u32 s96, s96, s58\n"
        "s_addc_u32 s97, s97, 0\n"
        "s_setpc_b64 s[96:97]\n"
        "s_nop 5\n"
        "s_nop 5\n"
        "s_nop 5\n"
        "s_nop 5\n"
        "s_nop 5\n"
        "LH_F_E:\n"
        "LH_TGO:\n"
        "s_mov_b32 s57, 0\n"
        "buffer_load_dwordx4 v[104:107], v1, s[40:43], s49 offen sc1\n"
        "buffer_load_dwordx4 v[108:111], v1, s[40:43], s50 offen sc1\n"
        "LH_POLL:\n"
        "s_waitcnt vmcnt(1)\n"
        "ds_write_b128 v1, v[104:107] offset:0\n"
        "v_max3_u32 v32, v104, v105, v106\n"
        "s_waitcnt vmcnt(0)\n"
        "ds_write_b128 v1, v[108:111] offset:4096\n"
        "v_max3_u32 v33, v108, v109, v110\n"
        "v_max3_u32 v32, v32, v107, v111\n"
        "v_max_u32 v32, v32, v33\n"
        "v_cmp_eq_u32 vcc, -1, v32\n"
        "s_add_u32 s57, s57, 1\n"
        "s_cbranch_vccz LH_TOP\n"
        "buffer_load_dwordx4 v[104:107], v1, s[40:43], s49 offen sc1\n"
        "buffer_load_dwordx4 v[108:111], v1, s[40:43], s50 offen sc1\n"
        "s_cmp_lt_u32 s57, 400000\n"
        "s_cbranch_scc1 LH_POLL\n"
        "s_branch LH_ABORT\n"
        "LH_GOT:\n"
        "s_not_b64 vcc, s[62:63]\n"
        "s_branch LH_TOP\n"
        "LH_FINB:\n"
        "s_waitcnt lgkmcnt(0)\n"
        "s_barrier\n"
        "LH_FIN:\n"
        "ds_read_b128 v[24:27], v13 offset:0\n"
        "ds_read_b128 v[28:31], v13 offset:2048\n"
        "ds_read_b128 v[32:35], v14 offset:0\n"
        "ds_read_b128 v[36:39], v14 offset:2048\n"
        "ds_read_b128 v[40:43], v13 offset:4096\n"
        "ds_read_b128 v[44:47], v13 offset:6144\n"
        "ds_read_b128 v[48:51], v14 offset:4096\n"
        "ds_read_b128 v[52:55], v14 offset:6144\n"
        "s_lshl_b32 s55, s44, 14\n"
        "s_add_u32 s55, s55, s56\n"
        "s_add_u32 s55, s55, 135168\n"
        "s_lshl_b32 s54, s45, 9\n"
        "s_add_u32 s54, s54, s55\n"
        "s_sub_u32 s54, s54, 4096\n"
        "s_cmp_lg_u32 s79, 0\n"
        "s_cbranch_scc1 LH_LV\n"
        "s_waitcnt lgkmcnt(7)\n"
        "v_smfmac_f32_16x16x64_bf16 v[64:67], v[24:27], a[128:135], v3\n"
        "v_smfmac_f32_16x16x64_bf16 v[68:71], v[24:27], a[192:199], v3\n"
        "s_waitcnt lgkmcnt(6)\n"
        "v_smfmac_f32_16x16x64_bf16 v[64:67], v[28:31], a[136:143], v3\n"
        "v_smfmac_f32_16x16x64_bf16 v[68:71], v[28:31], a[200:207], v3\n"
        "s_waitcnt lgkmcnt(5)\n"
        "v_smfmac_f32_16x16x64_bf16 v[64:67], v[32:35], a[144:151], v3\n"
        "v_smfmac_f32_16x16x64_bf16 v[68:71], v[32:35], a[208:215], v3\n"
        "s_waitcnt lgkmcnt(4)\n"
        "v_smfmac_f32_16x16x64_bf16 v[64:67], v[36:39], a[152:159], v3\n"
        "v_smfmac_f32_16x16x64_bf16 v[68:71], v[36:39], a[216:223], v3\n"
        "s_waitcnt lgkmcnt(3)\n"
        "v_smfmac_f32_16x16x64_bf16 v[64:67], v[40:43], a[160:167], v3\n"
        "v_smfmac_f32_16x16x64_bf16 v[68:71], v[40:43], a[224:231], v3\n"
        "s_waitcnt lgkmcnt(2)\n"
        "v_smfmac_f32_16x16x64_bf16 v[64:67], v[44:47], a[168:175], v3\n"
        "v_smfmac_f32_16x16x64_bf16 v[68:71], v[44:47], a[232:239], v3\n"
        "s_waitcnt lgkmcnt(1)\n"
        "v_smfmac_f32_16x16x64_bf16 v[64:67], v[48:51], a[176:183], v3\n"
        "v_smfmac_f32_16x16x64_bf16 v[68:71], v[48:51], a[240:247], v3\n"
        "s_waitcnt lgkmcnt(0)\n"
        "v_smfmac_f32_16x16x64_bf16 v[64:67], v[52:55], a[184:191], v3\n"
        "v_smfmac_f32_16x16x64_bf16 v[68:71], v[52:55], a[248:255], v3\n"
        "s_waitcnt vmcnt(0)\n"
        "v_mov_b64 v[104:105], 0\n"
        "s_nop 0\n"
        "v_add_f32 v64, v64, v65\n"
        "v_add_f32 v65, v66, v67\n"
        "s_nop 1\n"
        "v_add_f32 v68, v68, v69\n"
        "v_add_f32 v69, v70, v71\n"
        "v_cvt_pk_bf16_f32 v106, v64, v65\n"
        "v_cvt_pk_bf16_f32 v107, v68, v69\n"
        "s_nop 1\n"
        "v_mfma_f32_16x16x32_bf16 v[56:59], v[104:107], v[112:115], 0\n"
        "v_mfma_f32_16x16x32_bf16 v[64:67], v[104:107], v[116:119], 0\n"
        "s_nop 3\n"
        "v_cvt_pk_bf16_f32 v20, v56, v57\n"
        "v_cvt_pk_bf16_f32 v21, v58, v59\n"
        "s_nop 1\n"
        "v_cvt_pk_bf16_f32 v22, v64, v65\n"
        "v_cvt_pk_bf16_f32 v23, v66, v67\n"
        "buffer_store_dwordx4 v[20:23], v11, s[40:43], s54 offen sc1\n"
        "s_branch LH_END\n"
        "LH_LV:\n"
        "s_waitcnt lgkmcnt(7)\n"
        "v_smfmac_f32_16x16x64_bf16 v[72:75], v[24:27], v[128:135], v3\n"
        "v_smfmac_f32_16x16x64_bf16 v[76:79], v[24:27], v[192:199], v3\n"
        "s_waitcnt lgkmcnt(6)\n"
        "v_smfmac_f32_16x16x64_bf16 v[72:75], v[28:31], v[136:143], v3\n"
        "v_smfmac_f32_16x16x64_bf16 v[76:79], v[28:31], v[200:207], v3\n"
        "s_waitcnt lgkmcnt(5)\n"
        "v_smfmac_f32_16x16x64_bf16 v[72:75], v[32:35], v[144:151], v3\n"
        "v_smfmac_f32_16x16x64_bf16 v[76:79], v[32:35], v[208:215], v3\n"
        "s_waitcnt lgkmcnt(4)\n"
        "v_smfmac_f32_16x16x64_bf16 v[72:75], v[36:39], v[152:159], v3\n"
        "v_smfmac_f32_16x16x64_bf16 v[76:79], v[36:39], v[216:223], v3\n"
        "s_waitcnt lgkmcnt(3)\n"
        "v_smfmac_f32_16x16x64_bf16 v[72:75], v[40:43], v[160:167], v3\n"
        "v_smfmac_f32_16x16x64_bf16 v[76:79], v[40:43], v[224:231], v3\n"
        "s_waitcnt lgkmcnt(2)\n"
        "v_smfmac_f32_16x16x64_bf16 v[72:75], v[44:47], v[168:175], v3\n"
        "v_smfmac_f32_16x16x64_bf16 v[76:79], v[44:47], v[232:239], v3\n"
        "s_waitcnt lgkmcnt(1)\n"
        "v_smfmac_f32_16x16x64_bf16 v[72:75], v[48:51], v[176:183], v3\n"
        "v_smfmac_f32_16x16x64_bf16 v[76:79], v[48:51], v[240:247], v3\n"
        "s_waitcnt lgkmcnt(0)\n"
        "v_smfmac_f32_16x16x64_bf16 v[72:75], v[52:55], v[184:191], v3\n"
        "v_smfmac_f32_16x16x64_bf16 v[76:79], v[52:55], v[248:255], v3\n"
        "s_nop 3\n"
        "v_add_f32 v72, v72, v73\n"
        "v_add_f32 v73, v74, v75\n"
        "s_nop 1\n"
        "v_add_f32 v76, v76, v77\n"
        "v_add_f32 v77, v78, v79\n"
        "v_cvt_pk_bf16_f32 v18, v72, v73\n"
        "v_cvt_pk_bf16_f32 v19, v76, v77\n"
        "buffer_store_dwordx2 v[18:19], v10, s[40:43], s55 offen sc1\n"
        "s_branch LH_END\n"
        "LH_ABORT:\n"
        "v_mov_b32 v16, 61440\n"
        "v_mov_b32 v17, s46\n"
        "v_or_b32 v17, 0x100, v17\n"
        "buffer_store_dword v17, v16, s[40:43], 0 offen sc1\n"
        "LH_END:\n"
        "s_waitcnt vmcnt(0) lgkmcnt(0)\n"
        :
        : [wslo] "s"((unsigned)ws), [wshi] "s"((unsigned)(ws >> 32)), [wqlo] "s"((unsigned)wq), [wqhi] "s"((unsigned)(wq >> 32)),
          [wklo] "s"((unsigned)wk), [wkhi] "s"((unsigned)(wk >> 32)), [wvlo] "s"((unsigned)wv), [wvhi] "s"((unsigned)(wv >> 32)),
          [wolo] "s"((unsigned)wo), [wohi] "s"((unsigned)(wo >> 32)), [mem0] "s"(p.mem0), [h] "s"(h), [c2] "s"(c2), [w] "s"(w)
        : "memory", "vcc", "scc", "v0", "v1", "v2", "v3", "v4", "v5", "v6", "v7", "v8", "v9", "v10", "v11", "v12", "v13", "v14", "v15", "v16", "v17", "v18", "v19", "v20", "v21", "v22", "v23", "v24", "v25", "v26", "v27", "v28", "v29", "v30", "v31", "v32", "v33", "v34", "v35", "v36", "v37", "v38", "v39", "v40", "v41", "v42", "v43", "v44", "v45", "v46", "v47", "v48", "v49", "v50", "v51", "v52", "v53", "v54", "v55", "v56", "v57", "v58", "v59", "v60", "v61", "v62", "v63", "v64", "v65", "v66", "v67", "v68", "v69", "v70", "v71", "v72", "v73", "v74", "v75", "v76", "v77", "v78", "v79", "v80", "v81", "v82", "v83", "v84", "v85", "v86", "v87", "v88", "v89", "v90", "v91", "v92", "v93", "v94", "v95", "v96", "v97", "v98", "v99", "v100", "v101", "v102", "v103", "v104", "v105", "v106", "v107", "v108", "v109", "v110", "v111", "v112", "v113", "v114", "v115", "v116", "v117", "v118", "v119", "v120", "v121", "v122", "v123", "v124", "v125", "v126", "v127", "v128", "v129", "v130", "v131", "v132", "v133", "v134", "v135", "v136", "v137", "v138", "v139", "v140", "v141", "v142", "v143", "v144", "v145", "v146", "v147", "v148", "v149", "v150", "v151", "v152", "v153", "v154", "v155", "v156", "v157", "v158", "v159", "v160", "v161", "v162", "v163", "v164", "v165", "v166", "v167", "v168", "v169", "v170", "v171", "v172", "v173", "v174", "v175", "v176", "v177", "v178", "v179", "v180", "v181", "v182", "v183", "v184", "v185", "v186", "v187", "v188", "v189", "v190", "v191", "v192", "v193", "v194", "v195", "v196", "v197", "v198", "v199", "v200", "v201", "v202", "v203", "v204", "v205", "v206", "v207", "v208", "v209", "v210", "v211", "v212", "v213", "v214", "v215", "v216", "v217", "v218", "v219", "v220", "v221", "v222", "v223", "v224", "v225", "v226", "v227", "v228", "v229", "v230", "v231", "v232", "v233", "v234", "v235", "v236", "v237", "v238", "v239", "v240", "v241", "v242", "v243", "v244", "v245", "v246", "v247", "v248", "v249", "v250", "v251", "v252", "v253", "v254", "v255", "a0", "a1", "a2", "a3", "a4", "a5", "a6", "a7", "a8", "a9", "a10", "a11", "a12", "a13", "a14", "a15", "a16", "a17", "a18", "a19", "a20", "a21", "a22", "a23", "a24", "a25", "a26", "a27", "a28", "a29", "a30", "a31", "a32", "a33", "a34", "a35", "a36", "a37", "a38", "a39", "a40", "a41", "a42", "a43", "a44", "a45", "a46", "a47", "a48", "a49", "a50", "a51", "a52", "a53", "a54", "a55", "a56", "a57", "a58", "a59", "a60", "a61", "a62", "a63", "a64", "a65", "a66", "a67", "a68", "a69", "a70", "a71", "a72", "a73", "a74", "a75", "a76", "a77", "a78", "a79", "a80", "a81", "a82", "a83", "a84", "a85", "a86", "a87", "a88", "a89", "a90", "a91", "a92", "a93", "a94", "a95", "a96", "a97", "a98", "a99", "a100", "a101", "a102", "a103", "a104", "a105", "a106", "a107", "a108", "a109", "a110", "a111", "a112", "a113", "a114", "a115", "a116", "a117", "a118", "a119", "a120", "a121", "a122", "a123", "a124", "a125", "a126", "a127", "a128", "a129", "a130", "a131", "a132", "a133", "a134", "a135", "a136", "a137", "a138", "a139", "a140", "a141", "a142", "a143", "a144", "a145", "a146", "a147", "a148", "a149", "a150", "a151", "a152", "a153", "a154", "a155", "a156", "a157", "a158", "a159", "a160", "a161", "a162", "a163", "a164", "a165", "a166", "a167", "a168", "a169", "a170", "a171", "a172", "a173", "a174", "a175", "a176", "a177", "a178", "a179", "a180", "a181", "a182", "a183", "a184", "a185", "a186", "a187", "a188", "a189", "a190", "a191", "a192", "a193", "a194", "a195", "a196", "a197", "a198", "a199", "a200", "a201", "a202", "a203", "a204", "a205", "a206", "a207", "a208", "a209", "a210", "a211", "a212", "a213", "a214", "a215", "a216", "a217", "a218", "a219", "a220", "a221", "a222", "a223", "a224", "a225", "a226", "a227", "a228", "a229", "a230", "a231", "a232", "a233", "a234", "a235", "a236", "a237", "a238", "a239", "a240", "a241", "a242", "a243", "a244", "a245", "a246", "a247", "a248", "a249", "a250", "a251", "a252", "a253", "a254", "a255", "s40", "s41", "s42", "s43", "s44", "s45", "s46", "s47", "s48", "s49", "s50", "s51", "s52", "s53", "s54", "s55", "s56", "s57", "s58", "s59", "s60", "s61", "s62", "s63", "s64", "s65", "s66", "s67", "s68", "s69", "s70", "s71", "s72", "s73", "s74", "s75", "s76", "s77", "s78", "s79", "s80", "s81", "s82", "s83", "s84", "s85", "s86", "s87", "s88", "s89", "s90", "s91", "s92", "s93", "s94", "s95", "s96", "s97", "s98", "s99", "s24", "s25", "s26", "s27", "s28", "s29", "s30", "s31");
}

template <int QM, bool QUIET = false>
__device__ __forceinline__ bool t_task(int s, int h, unsigned kmb, unsigned vmb, rsrc_t ws, unsigned tag, unsigned& hprog, LAS unsigned char* lds = nullptr) {
    const int lane = threadIdx.x & 63, g = lane >> 4, c = lane & 15;
    constexpr bool Q0 = QM & 1, Q1 = QM & 2;
    const float c1 = 0.08838834764831845f * 1.4426950408889634f, NINF = -__builtin_inff();
    const unsigned blk = h * 524288 + s * 8192;
    v4u qf[4][2], kf[4][2]; v2u vf[4][2][2];
#pragma unroll
    for (int k = 0; k < 4; ++k) {
        if (Q0) qf[k][0] = ldg16(ws, WS_QX + blk + k * 2048 + lane * 16);
        if (Q1) qf[k][1] = ldg16(ws, WS_QX + blk + k * 2048 + 1024 + lane * 16);
        kf[k][0] = ldg16(ws, WS_KX + blk + k * 2048 + lane * 16);
        if (Q1) kf[k][1] = ldg16(ws, WS_KX + blk + k * 2048 + 1024 + lane * 16);
    }
#pragma unroll
    for (int k = 0; k < 4; ++k)
#pragma unroll
        for (int te = 0; te < 2; ++te)
#pragma unroll
            for (int v = 0; v < 2; ++v) vf[k][te][v] = ldg8(ws, WS_VX + blk + k * 2048 + te * 1024 + (g >> 1) * 512 + ((2 * (g & 1) + v) * 16 + c) * 8);
    v4f S[5] = {};
#pragma unroll
    for (int k = 0; k < 4; ++k) { if (Q0) S[0] = mfma32(kf[k][0], qf[k][0], S[0]); if (Q1) { S[1] = mfma32(kf[k][0], qf[k][1], S[1]); S[2] = mfma32(kf[k][1], qf[k][1], S[2]); } }
#pragma unroll
    for (int r = 0; r < 4; ++r) if (4 * g + r > c) { S[0][r] = NINF; S[2][r] = NINF; }
    float m0 = fmaxf(fmaxf(S[0][0], S[0][1]), fmaxf(S[0][2], S[0][3]));
    float m1 = fmaxf(fmaxf(fmaxf(S[1][0], S[1][1]), fmaxf(S[1][2], S[1][3])), fmaxf(fmaxf(S[2][0], S[2][1]), fmaxf(S[2][2], S[2][3])));
    if (Q0) asm volatile("" : "+v"(m0));
    if (Q1) asm volatile("" : "+v"(m1));
#pragma unroll
    for (int k = 0; k < 4; ++k) asm volatile("" : "+v"(vf[k][0][0]), "+v"(vf[k][0][1]), "+v"(vf[k][1][0]), "+v"(vf[k][1][1]));
    if (s < 60 && hprog < (unsigned)(s + 1)) { hprog = wait_flag2(ws, WS_HPROG + h * 128 + (lane & 1) * 8192, tag, s + 1, 0x605); if (!hprog) return false; }
    if (QM != 3) TR(32, RT());
    v4u km[4]; v2u va[4], vb[4];
    if (QUIET) { for (unsigned sp = 0; rfl(*(volatile LAS unsigned*)(lds + 64)) < 63u; ++sp) { __builtin_amdgcn_s_sleep(1); if (sp > SPIN) return false; } }
    if (s >= 60) {
        const unsigned co = lane < 16 ? kmb + lane * 128 : vmb + ((lane >> 2) & 3) * 1024 + (lane & 3) * 128;
        for (unsigned sp = 0; __any(ldg4(ws, co) == SENT); ++sp) { asm volatile("" ::: "memory"); if (sp > SPIN) { stg4(0x609, ws, WS_ABORT); return false; } }
    }
    for (unsigned sp = 0;; ++sp) {
        asm volatile("" ::: "memory");
#pragma unroll
        for (int k = 0; k < 4; ++k) km[k] = ldg16(ws, kmb + k * 512 + g * 128 + (c & 7) * 16);
#pragma unroll
        for (int k = 0; k < 4; ++k) { va[k] = ldg8(ws, vmb + k * 1024 + ((g & 1) * 16 + c) * 8); vb[k] = ldg8(ws, vmb + k * 1024 + ((2 + (g & 1)) * 16 + c) * 8); }
        if (s < 60) break;
        bool miss = false;
#pragma unroll
        for (int k = 0; k < 4; ++k) miss |= km[k].x == SENT || km[k].y == SENT || km[k].z == SENT || km[k].w == SENT || va[k].x == SENT || va[k].y == SENT || vb[k].x == SENT || vb[k].y == SENT;
        if (!__any(miss)) break;
        if (sp > SPIN) { stg4(0x607, ws, WS_ABORT); return false; }
    }
    if (QM != 3) TR(33, RT());
#pragma unroll
    for (int k = 0; k < 4; ++k) { if (Q0) S[3] = mfma32(km[k], qf[k][0], S[3]); if (Q1) S[4] = mfma32(km[k], qf[k][1], S[4]); }
#pragma unroll
    for (int r = 0; r < 4; ++r) if (g < 2) { S[3][r] = NINF; S[4][r] = NINF; }
    v4u pf0, pf1; v2u pm0, pm1;
    if (Q0) {
        m0 = x32max(x16max(fmaxf(m0, fmaxf(fmaxf(S[3][0], S[3][1]), fmaxf(S[3][2], S[3][3])))));
        const float n0 = -m0 * c1; float l0 = 0.f;
#pragma unroll
        for (int r = 0; r < 4; ++r) { S[0][r] = __builtin_amdgcn_exp2f(fmaf(S[0][r], c1, n0)); S[3][r] = __builtin_amdgcn_exp2f(fmaf(S[3][r], c1, n0)); l0 += S[0][r] + S[3][r]; }
        l0 = __builtin_amdgcn_rcpf(x32add(x16add(l0)));
        pf0 = (v4u){pk(S[0][0] * l0, S[0][1] * l0), 0u, pk(S[0][2] * l0, S[0][3] * l0), 0u};
        pm0 = (v2u){pk(S[3][0] * l0, S[3][1] * l0), pk(S[3][2] * l0, S[3][3] * l0)};
    }
    if (Q1) {
        m1 = x32max(x16max(fmaxf(m1, fmaxf(fmaxf(S[4][0], S[4][1]), fmaxf(S[4][2], S[4][3])))));
        const float n1 = -m1 * c1; float l1 = 0.f;
#pragma unroll
        for (int r = 0; r < 4; ++r) { S[1][r] = __builtin_amdgcn_exp2f(fmaf(S[1][r], c1, n1)); S[2][r] = __builtin_amdgcn_exp2f(fmaf(S[2][r], c1, n1)); S[4][r] = __builtin_amdgcn_exp2f(fmaf(S[4][r], c1, n1)); l1 += (S[1][r] + S[2][r]) + S[4][r]; }
        l1 = __builtin_amdgcn_rcpf(x32add(x16add(l1)));
        pf1 = (v4u){pk(S[1][0] * l1, S[1][1] * l1), pk(S[2][0] * l1, S[2][1] * l1), pk(S[1][2] * l1, S[1][3] * l1), pk(S[2][2] * l1, S[2][3] * l1)};
        pm1 = (v2u){pk(S[4][0] * l1, S[4][1] * l1), pk(S[4][2] * l1, S[4][3] * l1)};
    }
    const unsigned ob = WS_OX + (s * 32 + h) * 8192 + lane * 16;
#pragma unroll
    for (int k = 0; k < 4; ++k) {
        v4f O[2][2];
#pragma unroll
        for (int te = 0; te < 2; ++te) {
            const v2u vm = {te ? va[k].y : va[k].x, te ? vb[k].y : vb[k].x};
            const v4u vx = {vf[k][te][0].x, vf[k][te][0].y, vf[k][te][1].x, vf[k][te][1].y};
            if (Q0) O[te][0] = mfma16(vm, pm0, mfma32(vx, pf0, (v4f){}));
            if (Q1) O[te][1] = mfma16(vm, pm1, mfma32(vx, pf1, (v4f){}));
        }
#pragma unroll
        for (int qt = 0; qt < 2; ++qt) if (qt ? Q1 : Q0) stg16((v4u){pk(O[0][qt][0], O[0][qt][1]), pk(O[0][qt][2], O[0][qt][3]), pk(O[1][qt][0], O[1][qt][1]), pk(O[1][qt][2], O[1][qt][3])}, ws, ob + k * 2048 + qt * 1024);
    }
    return true;
}
__device__ __forceinline__ unsigned probe_lat(rsrc_t ws, unsigned off) {
    unsigned long long t0, t1; unsigned v;
    asm volatile("s_memtime %0\n\ts_waitcnt lgkmcnt(0)" : "=s"(t0) :: "memory");
    v = ldg4(ws, off);
    asm volatile("s_waitcnt vmcnt(0)\n\ts_memtime %0\n\ts_waitcnt lgkmcnt(0)" : "=s"(t1) : "v"(v) : "memory");
    return (unsigned)(t1 - t0);
}
__device__ __forceinline__ void role_reducer(const Params& p, int r, int w, LAS unsigned char* lds, rsrc_t ws) {
    const int t = threadIdx.x, lane = t & 63;
    const unsigned tagv = ldg4(ws, WS_E);
    const v4u S = {~0u, ~0u, ~0u, ~0u}, S1 = {0x7F817F81u, 0x7F817F81u, 0x7F817F81u, 0x7F817F81u};
    if (t == 0) { *(volatile LAS unsigned*)lds = 0; *(volatile LAS unsigned*)(lds + 64) = 0; }
#pragma unroll
    for (int i = 0; i < 4; ++i) stg16(S1, ws, WS_INBOX + r * 16384 + i * 4096 + t * 16);
    if (t < 64) stg16(S, ws, WS_MEMBOX + (t >> 3) * 8192 + r * 128 + (t & 7) * 16);
    stg16(S1, ws, WS_MEMBOX + 131072 + r * 8192 + t * 16); stg16(S1, ws, WS_MEMBOX + 131072 + r * 8192 + 4096 + t * 16);
    const unsigned tag = rfl(tagv) + 1;
    DRAIN(); BARRIER();
    if (w == 0) {
        unsigned l0 = ~0u, l1 = ~0u;
        for (int i = 0; i < 6; ++i) { l0 = min(l0, probe_lat(ws, WS_INBOX + r * 16384 + i * 1152)); l1 = min(l1, probe_lat(ws, WS_INBOX + r * 16384 + 8192 + i * 1152)); }
        if (lane == 0) set_flag(ws, WS_RDY + r * 8, tag, min(l0, 65535u) | (min(l1, 65535u) << 16));
        TR(3, RT()); TR(4, RT());
        unsigned votes;
        for (unsigned sp = 0;; ++sp) { asm volatile("" ::: "memory"); const v2u f = ldg8(ws, WS_RDY + lane * 8); if (__all(f.y == tag)) { votes = f.x; break; } if (sp > SPIN) { stg4(0x300, ws, WS_ABORT); return; } __builtin_amdgcn_s_sleep(2); }
        unsigned b0 = votes & 0xffffu, b1 = votes >> 16;
#pragma unroll
        for (int m = 1; m < 64; m <<= 1) { b0 = min(b0, (unsigned)__shfl_xor((int)b0, m)); b1 = min(b1, (unsigned)__shfl_xor((int)b1, m)); }
        const unsigned ci = rfl(b1) < rfl(b0) ? 8192u : 0u, ctl = WS_MEMBOX + ci + 65536u;
        if (lane == 0) *(volatile LAS unsigned*)(lds + 128) = ci;
        const unsigned G = lane >> 4, j = lane & 15;
        const unsigned io = WS_INBOX + r * 16384 + ci + lane * 16;
        const unsigned po = j >= 8 ? 0x7ff00000u : WS_MEMBOX + ci + r * 128 + (j ^ (4 * ((r >> 1) & 1))) * 16 + (2 * (G & 1) + (G >> 1)) * 4;
        const unsigned one = 0x3f80u << (16 * (j & 1));
        const v4u sel = {(j >> 1) == 0 ? one : 0u, (j >> 1) == 1 ? one : 0u, (j >> 1) == 2 ? one : 0u, (j >> 1) == 3 ? one : 0u};
        const uint64_t wsa = (uint64_t)p.ws;
        asm volatile(
        "s_mov_b32 s40, %[wslo]\n"
        "s_and_b32 s41, %[wshi], 0xffff\n"
        "s_mov_b32 s42, 113246208\n"
        "s_mov_b32 s43, 0x20000\n"
        "s_mov_b32 s46, 0\n"
        "s_mov_b32 s51, 0\n"
        "s_mov_b32 s73, 0x7f817f81\n"
        "s_mov_b32 s79, 0\n"
        "s_mov_b64 s[74:75], 0\n"
        "s_lshl_b32 s54, %[tag], 6\n"
        "s_mov_b32 s55, 0\n"
        "v_mbcnt_lo_u32_b32 v89, -1, 0\n"
        "v_and_b32 v89, 3, v89\n"
        "v_lshlrev_b32 v89, 3, v89\n"
        "v_mov_b32 v91, 0\n"
        "v_not_b32 v91, %[tag]\n"
        "s_mov_b32 s53, 0\n"
        "s_mov_b32 s52, 0\n"
        "v_mov_b32 v94, 0\n"
        "v_mov_b32 v100, -1\n"
        "v_mov_b32 v104, s73\n"
        "v_mov_b32 v105, s73\n"
        "v_mov_b32 v106, s73\n"
        "v_mov_b32 v107, s73\n"
        "s_mov_b32 s64, 0x00ff00ff\n"
        "s_mov_b32 s65, 0x00ff00ff\n"
        "LR_STEP:\n"
        "s_and_b32 s58, s46, 3\n"
        "s_lshl_b32 s49, s58, 14\n"
        "s_xor_b32 s50, s49, 32768\n"
        "s_mov_b32 s57, 0\n"
        "s_sleep 8\n"
        "s_cmp_eq_u32 s46, 0\n"
        "s_cbranch_scc1 LR_TGO\n"
        "s_add_u32 s85, s54, s46\n"
        "LR_TC:\n"
        "buffer_load_dwordx2 v[92:93], v94, s[40:43], %[ctl] offen sc1\n"
        "s_add_u32 s57, s57, 1\n"
        "s_cmp_gt_u32 s57, 64\n"
        "s_cselect_b32 s57, 0, s57\n"
        "s_cbranch_scc1 LR_TZ\n"
        "s_waitcnt vmcnt(0)\n"
        "v_readfirstlane_b32 s58, v93\n"
        "v_readfirstlane_b32 s59, v92\n"
        "s_cmp_lg_u32 s58, s85\n"
        "s_cbranch_scc1 LR_TC\n"
        "s_mov_b32 s57, 0\n"
        "s_sub_u32 s59, s59, 54\n"
        "s_memrealtime s[82:83]\n"
        "s_waitcnt lgkmcnt(0)\n"
        "s_lshl_b32 s58, s82, 1\n"
        "s_sub_u32 s58, s59, s58\n"
        "s_ashr_i32 s58, s58, 1\n"
        "s_add_u32 s59, s82, s58\n"
        "s_cmp_gt_i32 s58, 400\n"
        "s_cbranch_scc1 LR_TZ\n"
        "LR_F_W:\n"
        "s_memrealtime s[82:83]\n"
        "s_waitcnt lgkmcnt(0)\n"
        "s_sub_u32 s58, s59, s82\n"
        "s_cmp_gt_i32 s58, 5\n"
        "s_cbranch_scc1 LR_F_W\n"
        "s_max_i32 s58, s58, 0\n"
        "s_lshl_b32 s58, s58, 2\n"
        "s_getpc_b64 s[82:83]\n"
        "LR_F_X:\n"
        "s_sub_u32 s58, LR_F_E-LR_F_X, s58\n"
        "s_add_u32 s82, s82, s58\n"
        "s_addc_u32 s83, s83, 0\n"
        "s_setpc_b64 s[82:83]\n"
        "s_nop 5\n"
        "s_nop 5\n"
        "s_nop 5\n"
        "s_nop 5\n"
        "s_nop 5\n"
        "LR_F_E:\n"
        "LR_TZ:\n"
        "LR_TGO:\n"
        "buffer_load_dwordx4 v[108:111], %[io], s[40:43], s51 offen offset:0 sc1\n"
        "buffer_load_dwordx4 v[112:115], %[io], s[40:43], s51 offen offset:1024 sc1\n"
        "buffer_load_dwordx4 v[116:119], %[io], s[40:43], s51 offen offset:2048 sc1\n"
        "buffer_load_dwordx4 v[120:123], %[io], s[40:43], s51 offen offset:3072 sc1\n"
        "LR_POLL:\n"
        "s_waitcnt vmcnt(3)\n"
        "v_mfma_f32_16x16x32_bf16 v[96:99], v[108:111], %[sel], 0\n"
        "s_waitcnt vmcnt(2)\n"
        "v_mfma_f32_16x16x32_bf16 v[96:99], v[112:115], %[sel], v[96:99]\n"
        "s_waitcnt vmcnt(1)\n"
        "v_mfma_f32_16x16x32_bf16 v[96:99], v[116:119], %[sel], v[96:99]\n"
        "s_waitcnt vmcnt(0)\n"
        "v_mfma_f32_16x16x32_bf16 v[96:99], v[120:123], %[sel], v[96:99]\n"
        "s_add_u32 s57, s57, 1\n"
        "s_nop 6\n"
        "v_permlane32_swap_b32 v96, v98\n"
        "v_permlane32_swap_b32 v97, v99\n"
        "v_add_f32 v96, v96, v98\n"
        "v_add_f32 v97, v97, v99\n"
        "v_cmp_u_f32 vcc, v96, v97\n"
        "v_cvt_pk_bf16_f32 v96, v96, v97\n"
        "v_min_u32 v96, -2, v96\n"
        "s_cbranch_vccnz LR_MISS\n"
        "LR_GOT:\n"
        "buffer_store_dword v96, %[po], s[40:43], s49 offen sc1\n"
        "s_memrealtime s[82:83]\n"
        "s_add_u32 s58, s54, s46\n"
        "s_add_u32 s58, s58, 1\n"
        "v_mov_b32 v93, s58\n"
        "s_cmp_eq_u32 s57, 1\n"
        "s_cselect_b32 s58, s46, 0\n"
        "s_cmp_gt_u32 s58, 1\n"
        "s_cbranch_scc1 LR_ONT\n"
        "s_cmp_le_u32 s46, s53\n"
        "s_cselect_b32 s58, 2, 0\n"
        "s_add_u32 s55, s55, s58\n"
        "s_add_u32 s53, s46, 5\n"
        "s_cmp_le_u32 s46, 4\n"
        "s_cselect_b32 s53, 0, s53\n"
        "s_waitcnt lgkmcnt(0)\n"
        "s_add_u32 s52, s82, 10\n"
        "s_mov_b32 s83, 0\n"
        "s_branch LR_ANN\n"
        "LR_ONT:\n"
        "s_waitcnt lgkmcnt(0)\n"
        "s_mov_b32 s83, 1\n"
        "LR_ANN:\n"
        "v_cmp_eq_u32 vcc, %[tag], v91\n"
        "s_nop 1\n"
        "v_cndmask_b32 v90, 0, v90, vcc\n"
        "s_and_b32 s59, vcc_lo, 15\n"
        "v_readlane_b32 s84, v90, 0\n"
        "v_readlane_b32 s85, v90, 1\n"
        "s_max_u32 s84, s84, s85\n"
        "v_readlane_b32 s85, v90, 2\n"
        "s_max_u32 s84, s84, s85\n"
        "v_readlane_b32 s85, v90, 3\n"
        "s_max_u32 s84, s84, s85\n"
        "s_add_u32 s84, s84, 1211\n"
        "s_lshr_b32 s84, s84, 4\n"
        "s_min_u32 s84, s84, 260\n"
        "s_cmp_eq_u32 s59, 15\n"
        "s_cselect_b32 s84, s84, 192\n"
        "s_add_u32 s84, s84, s55\n"
        "s_add_u32 s52, s52, s84\n"
        "s_mov_b32 s72, s84\n"
        "s_lshl1_add_u32 s83, s52, s83\n"
        "v_mov_b32 v92, s83\n"
        "buffer_store_dwordx2 v[92:93], v94, s[40:43], %[ctlw] offen sc1\n"
        "s_add_u32 s84, %[ctl], 16384\n"
        "buffer_load_dwordx2 v[90:91], v89, s[40:43], s84 offen sc1\n"
        "s_add_u32 s58, s46, 1\n"
        "v_mov_b32 v95, s58\n"
        "ds_write_b32 v94, v95 offset:64\n"
        "s_sleep 8\n"
        "buffer_store_dword v100, %[po], s[40:43], s50 offen sc1\n"
        "buffer_store_dwordx4 v[104:107], %[io], s[40:43], s51 offen offset:0 sc1\n"
        "buffer_store_dwordx4 v[104:107], %[io], s[40:43], s51 offen offset:1024 sc1\n"
        "buffer_store_dwordx4 v[104:107], %[io], s[40:43], s51 offen offset:2048 sc1\n"
        "buffer_store_dwordx4 v[104:107], %[io], s[40:43], s51 offen offset:3072 sc1\n"
        "s_xor_b32 s51, s51, 4096\n"
        "s_add_u32 s46, s46, 1\n"
        "s_cmp_lt_u32 s46, 63\n"
        "s_cbranch_scc1 LR_STEP\n"
        "s_branch LR_END\n"
        "LR_MISS:\n"
        "s_and_b32 s58, s57, 63\n"
        "s_cmp_lt_u32 s58, 63\n"
        "s_cbranch_scc1 LR_SKIP\n"
        "s_mov_b64 s[60:61], 0\n"
        "v_cmp_eq_u32 vcc, s73, v108\n"
        "s_or_b64 s[60:61], s[60:61], vcc\n"
        "v_cmp_eq_u32 vcc, s73, v109\n"
        "s_or_b64 s[60:61], s[60:61], vcc\n"
        "v_cmp_eq_u32 vcc, s73, v110\n"
        "s_or_b64 s[60:61], s[60:61], vcc\n"
        "v_cmp_eq_u32 vcc, s73, v111\n"
        "s_or_b64 s[60:61], s[60:61], vcc\n"
        "v_cmp_eq_u32 vcc, s73, v112\n"
        "s_or_b64 s[60:61], s[60:61], vcc\n"
        "v_cmp_eq_u32 vcc, s73, v113\n"
        "s_or_b64 s[60:61], s[60:61], vcc\n"
        "v_cmp_eq_u32 vcc, s73, v114\n"
        "s_or_b64 s[60:61], s[60:61], vcc\n"
        "v_cmp_eq_u32 vcc, s73, v115\n"
        "s_or_b64 s[60:61], s[60:61], vcc\n"
        "v_cmp_eq_u32 vcc, s73, v116\n"
        "s_or_b64 s[60:61], s[60:61], vcc\n"
        "v_cmp_eq_u32 vcc, s73, v117\n"
        "s_or_b64 s[60:61], s[60:61], vcc\n"
        "v_cmp_eq_u32 vcc, s73, v118\n"
        "s_or_b64 s[60:61], s[60:61], vcc\n"
        "v_cmp_eq_u32 vcc, s73, v119\n"
        "s_or_b64 s[60:61], s[60:61], vcc\n"
        "v_cmp_eq_u32 vcc, s73, v120\n"
        "s_or_b64 s[60:61], s[60:61], vcc\n"
        "v_cmp_eq_u32 vcc, s73, v121\n"
        "s_or_b64 s[60:61], s[60:61], vcc\n"
        "v_cmp_eq_u32 vcc, s73, v122\n"
        "s_or_b64 s[60:61], s[60:61], vcc\n"
        "v_cmp_eq_u32 vcc, s73, v123\n"
        "s_or_b64 s[60:61], s[60:61], vcc\n"
        "s_cbranch_scc0 LR_GOT\n"
        "LR_SKIP:\n"
        "buffer_load_dwordx4 v[108:111], %[io], s[40:43], s51 offen offset:0 sc1\n"
        "buffer_load_dwordx4 v[112:115], %[io], s[40:43], s51 offen offset:1024 sc1\n"
        "buffer_load_dwordx4 v[116:119], %[io], s[40:43], s51 offen offset:2048 sc1\n"
        "buffer_load_dwordx4 v[120:123], %[io], s[40:43], s51 offen offset:3072 sc1\n"
        "s_cmp_lt_u32 s57, 400000\n"
        "s_cbranch_scc1 LR_POLL\n"
        "LR_ABORT:\n"
        "v_mov_b32 v104, 61440\n"
        "v_mov_b32 v105, s46\n"
        "v_or_b32 v105, 0x200, v105\n"
        "buffer_store_dword v105, v104, s[40:43], 0 offen sc1\n"
        "LR_END:\n"
        "s_waitcnt vmcnt(0)\n"
            :
            : [wslo] "s"((unsigned)wsa), [wshi] "s"((unsigned)(wsa >> 32)), [io] "v"(io), [po] "v"(po), [sel] "v"(sel), [rid] "s"(r), [tag] "s"(tag), [ctl] "s"(ctl), [ctlw] "s"(r == 0 ? ctl : 0x7ff00000u)
            : "memory", "vcc", "scc", "v88", "v89", "v90", "v91", "v92", "v93", "v94", "v95", "v96", "v97", "v98", "v99", "v100", "v101", "v102", "v103", "v104", "v105", "v106", "v107", "v108", "v109", "v110", "v111", "v112", "v113", "v114", "v115", "v116", "v117", "v118", "v119", "v120", "v121", "v122", "v123", "v124", "v125", "v126", "v127", "v128", "v129", "v130", "v131", "v132", "v133", "v134", "v135", "v136", "v137", "v138", "v139", "v140", "v141", "v142", "v143", "v144", "v145", "v146", "v147", "v148", "v149", "v150", "v151", "v152", "v153", "v154", "v155", "v156", "v157", "v158", "v159", "v160", "v161", "v162", "v163", "v164", "v165", "v166", "v167", "v168", "v169", "v170", "v171", "v172", "v173", "v174", "v175", "v176", "v177", "v178", "v179", "v180", "v181", "v182", "v183", "v184", "v185", "v186", "v187", "v188", "v189", "v190", "v191", "v192", "v193", "v194", "v195", "v196", "v197", "v198", "v199", "v200", "v201", "v202", "v203", "s40", "s41", "s42", "s43", "s44", "s45", "s46", "s47", "s48", "s49", "s50", "s51", "s52", "s53", "s54", "s55", "s56", "s57", "s58", "s59", "s60", "s61", "s62", "s63", "s64", "s65", "s66", "s67", "s68", "s69", "s70", "s71", "s72", "s73", "s74", "s75", "s76", "s77", "s78", "s79", "s80", "s81", "s82", "s83", "s84", "s85", "s86", "s87", "s88", "s89", "s90", "s91");
    } else {
        if (w == 2) {
            const int h = (r >> 3) * 4 + (r & 3);
            for (unsigned sp = 0; rfl(*(volatile LAS unsigned*)(lds + 64)) < 60u; ++sp) { __builtin_amdgcn_s_sleep(2); if (sp > SPIN) return; }
            for (unsigned sp = 0;; ++sp) { asm volatile("" ::: "memory"); const v2u f3 = ldg8(ws, WS_PPROG + (min(lane, 2) * 32 + h) * 128); if (__all(f3.y == tag && f3.x == 64u)) break; if (sp > SPIN) return; __builtin_amdgcn_s_sleep(8); }
            asm volatile("" ::: "memory");
            const unsigned kb = WS_MEMBOX + 131072 + h * 16384 + rfl(*(volatile LAS unsigned*)(lds + 128));
            unsigned hp = 64;
            if (r & 4) t_task<2, true>(63, h, kb, kb + 4096, ws, tag, hp, lds); else t_task<1, true>(63, h, kb, kb + 4096, ws, tag, hp, lds);
            TR(64 + 63, RT());
        }
        if (2 && w == 1) {
            const int h = (r >> 3) * 4 + (r & 3), isv = (r >> 2) & 1;
            const unsigned base = (isv ? WS_VX : WS_KX) + h * 524288 + lane * 16, foff = WS_PPROG + ((1 + isv) * 32 + h) * 128;
            unsigned prog = 0;
            for (int s = 0; s < 64; ++s) {
                unsigned sp = 0;
                while (rfl(*(volatile LAS unsigned*)(lds + 64)) + 2 < (unsigned)s) { __builtin_amdgcn_s_sleep(2); if (++sp > SPIN) return; }
                while (prog <= (unsigned)s) {
                    asm volatile("" ::: "memory");
                    const v2u f = ldg8(ws, foff); prog = rfl(f.y) == tag ? rfl(f.x) : 0u;
                    if (prog > (unsigned)s || s > 2 + 1 || ++sp > SPIN) break;
                    __builtin_amdgcn_s_sleep(4);
                }
                if (prog <= (unsigned)s) continue;
                asm volatile("" ::: "memory");
                v4u a = {0, 0, 0, 0};
#pragma unroll
                for (int i = 0; i < 8; ++i) a ^= ldg16(ws, base + s * 8192 + i * 1024);
                asm volatile("" :: "v"(a));
            }
        }
    }
}

template <int M>
__device__ __forceinline__ void role_P(const Params& p, int u, int mq, int w, LAS unsigned char* lds, rsrc_t ws, unsigned& tag) {
    const int lane = threadIdx.x & 63, g = lane >> 4, c = lane & 15;
    const unsigned tagv = ldg4(ws, WS_E);
    constexpr unsigned RS = M < 3 ? 8192 : 3072;
    const v4u xs = mksrd(p.x, 2048 * 512 * 2), wsrd = mksrd(M >= 3 ? p.bqkv : (mq == 0 ? p.wq : (mq == 1 ? p.wk : p.wv)), 512 * RS);
    constexpr unsigned XB[3] = {131072, 0, 32768};
    unsigned xvo[8];
#pragma unroll
    for (int i = 0; i < 8; ++i) xvo[i] = (unsigned)((lane ^ ((w * 8 + i) & 15)) * 16);
    auto dma_x = [&](int s, unsigned xb) {
#pragma unroll
        for (int i = 0; i < 8; ++i) { const int row = w * 8 + i; dma16(xb + row * 1024, xvo[i], xs, (unsigned)((s * 32 + row) * 1024)); }
    };
    if (M >= 3 || (M == 0 && mq == 0)) { for (int i = 0; i < (M >= 3 ? 1 : 2); ++i) __builtin_amdgcn_s_sleep(127); }
    dma_x(0, XB[0]);
    {   const unsigned vo = g * RS + ((c ^ (g << 2)) * 16);
#pragma unroll
        for (int i = 0; i < 32; ++i) { const int row0 = w * 4 + i * 16; dma16(row0 * 256, vo, wsrd, (unsigned)(u * 256 + row0 * RS)); } }
    const int hf = lane >> 5, tk = lane & 31;
    v4u wf[32];
    {   const int q = (lane >> 2) & 3, pp = lane & 3;
        LAS unsigned char* b0 = lds + (8 * hf + q) * 256 + (((4 * w + 2 * (g & 1) + (pp >> 1)) ^ (q << 2)) * 16) + (pp & 1) * 8;
#pragma unroll
        for (int qd = 0; qd < 4; ++qd) {
            if (qd == 0) asm volatile("s_waitcnt vmcnt(24)\n\ts_barrier" ::: "memory"); else if (qd == 1) asm volatile("s_waitcnt vmcnt(16)\n\ts_barrier" ::: "memory");
            else if (qd == 2) asm volatile("s_waitcnt vmcnt(8)\n\ts_barrier" ::: "memory"); else asm volatile("s_waitcnt vmcnt(0)\n\ts_barrier" ::: "memory");
#pragma unroll
            for (int k = 0; k < 8; ++k) { const int ks = qd * 8 + k;
                const v2u a0 = trread(b0 + ks * 4096), a1 = trread(b0 + ks * 4096 + 1024);
                wf[ks] = (v4u){a0.x, a0.y, a1.x, a1.y}; }
        }
    }
    if (M < 3) {
        const v4u S1 = {SENT, SENT, SENT, SENT};
        const unsigned t16 = threadIdx.x * 16;
        if (M == 2) { for (int i = 0; i < 4; ++i) stg16(S1, ws, WS_VM + (u * 64 + 60 + i) * 4096 + t16); }
        else if (mq == 1) { stg16(S1, ws, WS_KM + (u * 64 + 60) * 2048 + t16); stg16(S1, ws, WS_KM + (u * 64 + 62) * 2048 + t16); }
        else {
            for (int i = 0; i < 4; ++i) stg16(S1, ws, WS_OX + ((62 + (i >> 1)) * 32 + u) * 8192 + (i & 1) * 4096 + t16);
        }
    }
    tag = rfl(tagv) + 1;
    BARRIER();
    dma_x(1, XB[1]);
    unsigned obase, foff;
    if (M < 3) { obase = (mq == 0 ? WS_QX : (mq == 1 ? WS_KX : WS_VX)) + u * 524288 + w * 2048; foff = WS_PPROG + (mq * 32 + u) * 128; }
    else { obase = (u < 4 ? WS_QB : (u < 8 ? WS_KB : WS_VB)) + (8 * (u & 3) + 2 * w) * 65536; foff = WS_PPROG + (96 + u) * 128; }
    constexpr int NDR = 3;
    constexpr bool TRN = M == 0 || M == 1 || M == 3;
    unsigned xcur = XB[0], xnxt = XB[1], xfree = XB[2];
    SEGDECL;
    for (int s = 0; s < 64; ++s) {
        if (s > 0) {
            if (s < 63) { if (M >= 2) asm volatile("s_waitcnt vmcnt(12)" ::: "memory"); else asm volatile("s_waitcnt vmcnt(10)" ::: "memory"); }
            else { if (M >= 2) asm volatile("s_waitcnt vmcnt(4)" ::: "memory"); else asm volatile("s_waitcnt vmcnt(2)" ::: "memory"); }
            SEG(0);
            BARRIER();
            SEG(1);
            if (s > NDR + 1 && threadIdx.x == 0) set_flag(ws, foff, tag, (unsigned)(s - 1));
        }
        v16f acc = {};
        LAS unsigned char* xb[8];
#pragma unroll
        for (int k = 0; k < 8; ++k) xb[k] = lds + xcur + tk * 1024 + (((2 * k + hf) ^ (tk & 15)) * 16);
        v4u xf[32];
        constexpr int PF = 4;
#pragma unroll
        for (int ks = 0; ks < PF; ++ks) xf[ks] = *(const LAS v4u*)(xb[ks & 7] + (ks >> 3) * 256);
        __builtin_amdgcn_sched_barrier(0);
        const bool pf = s + 2 < 64;
#pragma unroll
        for (int ks = 0; ks < 32; ++ks) {
            if (pf && (ks & 3) == 1) { const int row = w * 8 + (ks >> 2); dma16(xfree + row * 1024, xvo[ks >> 2], xs, (unsigned)(((s + 2) * 32 + row) * 1024)); }
            if (ks + PF < 32) xf[ks + PF] = *(const LAS v4u*)(xb[(ks + PF) & 7] + ((ks + PF) >> 3) * 256);
            __builtin_amdgcn_sched_barrier(0);
            acc = TRN ? mfmaW(wf[ks], xf[ks], acc) : mfmaW(xf[ks], wf[ks], acc);
            __builtin_amdgcn_sched_barrier(0);
        }
        SEG(2);
        if (M == 3 && u < 4) acc *= 0.25f * 1.4426950408889634f;
        unsigned pw[8];
#pragma unroll
        for (int i = 0; i < 8; ++i) pw[i] = pk(acc[2 * i], acc[2 * i + 1]);
        if (M == 2) {
            const unsigned ob = obase + s * 8192 + (tk >> 4) * 1024 + (tk & 15) * 8;
#pragma unroll
            for (int h2 = 0; h2 < 2; ++h2)
#pragma unroll
                for (int v = 0; v < 2; ++v) stg8((v2u){pw[2 * h2 + v], pw[4 + 2 * h2 + v]}, ws, ob + h2 * 512 + (2 * hf + v) * 128);
        } else if (M < 2) {
            const unsigned ob = obase + s * 8192 + (tk >> 4) * 1024 + (tk & 15) * 16;
            stg16((v4u){pw[0], pw[1], pw[4], pw[5]}, ws, ob + hf * 256);
            stg16((v4u){pw[2], pw[3], pw[6], pw[7]}, ws, ob + (2 + hf) * 256);
        } else {
            const unsigned ob = obase + s * 1024;
#pragma unroll
            for (int q4 = 0; q4 < 4; ++q4)
                stg8((v2u){pw[2 * q4], pw[2 * q4 + 1]}, ws, M == 3 ? ob + (q4 >> 1) * 65536 + (q4 & 1) * 512 + tk * 16 + hf * 8
                                                                    : ob + (tk >> 4) * 65536 + (q4 >> 1) * 512 + hf * 256 + (tk & 15) * 16 + (q4 & 1) * 8);
        }
        if (s < NDR) { DRAIN(); BARRIER(); if (threadIdx.x == 0) set_flag(ws, foff, tag, (unsigned)(s + 1)); }
        SEG(3);
        if (s == 0) TR(3, RT());
        const unsigned tmp = xcur; xcur = xnxt; xnxt = xfree; xfree = tmp;
    }
    SEGOUT(8);
    DRAIN(); BARRIER();
    if (threadIdx.x == 0) set_flag(ws, foff, tag, 64u);
}

constexpr int NB2 = 20;
struct BKQ { v4u q, k[9], warm[3]; };
__device__ __forceinline__ void b2_loadkq(BKQ& d, int qb, int hB, rsrc_t ws, int lane) {
    const unsigned hb = hB * 65536 + lane * 16;
    d.q = ldg16(ws, WS_QB + hb + qb * 1024);
#pragma unroll
    for (int kb = 0; kb < 9; ++kb) d.k[kb] = ldg16(ws, WS_KB + hb + max(qb - 8 + kb, 0) * 1024);
#pragma unroll
    for (int i = 0; i < 3; ++i) d.warm[i] = ldg16(ws, WS_VB + hb + max(qb - i, 0) * 1024);
}
__device__ __forceinline__ void role_B2(const Params& p, int j, int w, rsrc_t ws, unsigned& tag) {
    const int lane = threadIdx.x & 63, hf = lane >> 5, qi = lane & 31, wv = j * 4 + w;
    tag = rfl(ldg4(ws, WS_E)) + 1;
    const unsigned cb = WS_BCST + wv * 1024;
    { const unsigned o = (qi == 16 || qi == 20) ? 0x3f803f80u : 0u; stg16((v4u){o, o, o, o}, ws, cb + lane * 16); }
    if (1) {
        unsigned acc = 0;
        for (int i = wv * 64 + lane; i < 4096 * 8; i += NB2 * 256) acc ^= ((const GAS unsigned*)p.wo)[i * 32];
        asm volatile("" :: "v"(acc));
    }
    {
        for (int i = 0; i < 1; ++i) __builtin_amdgcn_s_sleep(127);
        const v4u gw = ((const GAS v4u*)p.gate)[lane];
        for (int row0 = wv; row0 < 2048; row0 += NB2 * 4 * 13) {
            v4u xv[13];
#pragma unroll
            for (int i = 0; i < 13; ++i) xv[i] = ((const GAS v4u*)p.x)[min(row0 + i * NB2 * 4, 2047) * 64 + lane];
            float mine = 0.f;
#pragma unroll
            for (int i = 0; i < 13; ++i) {
                float s = 0.f;
#pragma unroll
                for (int k = 0; k < 4; ++k) { s += fb(xv[i][k] << 16) * fb(gw[k] << 16); s += fb(xv[i][k] & 0xffff0000u) * fb(gw[k] & 0xffff0000u); }
#pragma unroll
                for (int m = 1; m < 16; m <<= 1) s += __shfl_xor(s, m);
                s = x32add(x16add(s));
                if (lane == i) mine = s;
            }
            const int row = row0 + lane * NB2 * 4;
            if (lane < 13 && row < 2048) stg4(ub(1.0f / (1.0f + __expf(-mine))), ws, WS_GBUF + row * 4);
        }
    }
    DRAIN();
    unsigned prog = 0;
    auto need = [&](int task) -> bool {
        const unsigned n = (unsigned)(task >> 3) + 1;
        for (unsigned sp = 0; prog < n; ++sp) {
            asm volatile("" ::: "memory");
            const v2u f = ldg8(ws, WS_PPROG + (96 + min(lane, 11)) * 128);
            unsigned v = f.y == tag ? f.x : 0u;
#pragma unroll
            for (int m = 1; m < 16; m <<= 1) v = min(v, (unsigned)__shfl_xor((int)v, m));
            prog = rfl(v);
            if (prog >= n) break;
            if (sp > SPIN) { stg4(0x400, ws, WS_ABORT); return false; }
            __builtin_amdgcn_s_sleep(4);
        }
        asm volatile("" ::: "memory");
        return true;
    };
    const float NINF = -__builtin_inff();
    const unsigned vmul = qi < 16 ? 1u : 0u, vlane = qi < 16 ? WS_VB + hf * 256 + qi * 16 : cb + lane * 16;
    BKQ d;
    const int lw = (j >> 2) * 4 + w, h8 = (j & 3) * 8;
    if (!need(lw)) return;
    b2_loadkq(d, lw >> 3, h8 + (lw & 7), ws, lane);
    unsigned ndone = 0;
    for (int task = lw; task < 512; task += NB2) {
        const int qb = task >> 3, hB = h8 + (task & 7);
        asm volatile("" :: "v"(d.warm[0]), "v"(d.warm[1]), "v"(d.warm[2]));
        v4u v[18];
#pragma unroll
        for (int kb = 0; kb < 9; ++kb) {
            const unsigned o = vlane + vmul * (unsigned)(hB * 65536 + max(qb - 8 + kb, 0) * 1024);
            v[2 * kb] = ldg16(ws, o); v[2 * kb + 1] = ldg16(ws, o + vmul * 512);
        }
        v16f S[9];
#pragma unroll
        for (int kb = 0; kb < 9; ++kb) S[kb] = mfmaW(d.k[kb], d.q, (v16f){});
        asm volatile("s_waitcnt vmcnt(18)" ::: "memory");
        if (ndone && lane == 0) set_flag(ws, WS_BPROG + wv * 64, tag, ndone);
        const int nxt = task + NB2;
        __builtin_amdgcn_sched_barrier(0);
        if (nxt < 512) { if (!need(nxt)) return; b2_loadkq(d, nxt >> 3, h8 + (nxt & 7), ws, lane); }
#pragma unroll
        for (int r = 0; r < 16; ++r) { const int ki = (r & 3) + 8 * (r >> 2) + 4 * hf;
            if (!(ki > qi)) S[0][r] = NINF;
            if (!(ki <= qi)) S[8][r] = NINF; }
        if (qb < 8) {
#pragma unroll
            for (int kb = 0; kb < 8; ++kb) if (qb - 8 + kb < 0) {
#pragma unroll
                for (int r = 0; r < 16; ++r) S[kb][r] = NINF; }
        }
        float m = S[8][0];
#pragma unroll
        for (int kb = 0; kb < 9; ++kb)
#pragma unroll
            for (int r = 0; r < 16; r += 2) m = fmaxf(fmaxf(m, S[kb][r]), S[kb][r + 1]);
        m = x32max(m);
        v16f o = {};
#pragma unroll
        for (int kb = 0; kb < 9; ++kb) {
            unsigned pp[8];
            const v16f D = S[kb] - m;
#pragma unroll
            for (int r = 0; r < 16; r += 2) pp[r >> 1] = pk(__builtin_amdgcn_exp2f(D[r]), __builtin_amdgcn_exp2f(D[r + 1]));
#pragma unroll
            for (int s = 0; s < 2; ++s) o = mfmaW(v[2 * kb + s], (v4u){pp[4 * s], pp[4 * s + 1], pp[4 * s + 2], pp[4 * s + 3]}, o);
        }
        const float inv = 1.0f / o[8];
        const unsigned ob = WS_OB + (unsigned)(((hB * 128 + 2 * qb + (qi >> 4)) * 64 + (qi & 15)) * 8);
        stg8((v2u){pk(o[0] * inv, o[1] * inv), pk(o[2] * inv, o[3] * inv)}, ws, ob + hf * 128);
        stg8((v2u){pk(o[4] * inv, o[5] * inv), pk(o[6] * inv, o[7] * inv)}, ws, ob + (2 + hf) * 128);
        ++ndone;
    }
    DRAIN();
    if (lane == 0) set_flag(ws, WS_BPROG + wv * 64, tag, 1000u);
}

__device__ __forceinline__ void role_T(const Params& p, int h, int w, rsrc_t ws, unsigned tag) {
    const int lane = threadIdx.x & 63;
    unsigned pprog = 0;
    auto need = [&](int s) -> bool {
        for (unsigned sp = 0; pprog <= (unsigned)s; ++sp) {
            asm volatile("" ::: "memory");
            const v2u f3 = ldg8(ws, WS_PPROG + (min(lane, 2) * 32 + h) * 128);
            const unsigned x = f3.y == tag ? f3.x : 0u;
            pprog = min(min((unsigned)__builtin_amdgcn_readlane((int)x, 0), (unsigned)__builtin_amdgcn_readlane((int)x, 1)), (unsigned)__builtin_amdgcn_readlane((int)x, 2));
            if (pprog > (unsigned)s) break;
            if (sp > SPIN) { stg4(0x604, ws, WS_ABORT); return false; }
            __builtin_amdgcn_s_sleep(8);
        }
        asm volatile("" ::: "memory");
        return true;
    };
    TR(4, RT());
    unsigned hprog = 0;
    for (int s = w; s < 62; s += 4) {
        if (!need(s) || !t_task<3>(s, h, WS_KM + (h * 64 + s) * 2048, WS_VM + (h * 64 + s) * 4096, ws, tag, hprog)) return;
        DRAIN();
        if (lane == 0) set_flag(ws, WS_TPROG + (w * 32 + h) * 64, tag, (unsigned)(s >> 2) + 1);
        TR(64 + s, RT());
    }
    if (!need(63)) return;
    if (w == 2) t_task<1>(62, h, WS_KM + (h * 64 + 62) * 2048, WS_VM + (h * 64 + 62) * 4096, ws, tag, hprog);
    else if (w == 3) t_task<2>(62, h, WS_KM + (h * 64 + 62) * 2048, WS_VM + (h * 64 + 62) * 4096, ws, tag, hprog);
    TR(64 + 62, RT());
}

template <int N> struct IC { static constexpr int value = N; };
constexpr int NGRP = 6, NEARLY = 24;
__device__ __forceinline__ void role_W(const Params& p, int grp, int ct, int w, LAS unsigned char* lds, rsrc_t ws, unsigned tag) {
    const int t = threadIdx.x, lane = t & 63, g = lane >> 4, c = lane & 15;
    BARRIER();
    v4u wof[9][4][2];
    {   const v4u s1 = mksrd(p.wo, 4096 * 1024), s2 = mksrd(p.bwo, 512 * 1024);
        const unsigned vo = (lane >> 2) * 1024 + (lane & 3) * 16 + ct * 64, lb = w * 40960;
        LAS unsigned char* b = lds + lb + (4 * g + ((lane >> 2) & 3)) * 64 + (lane & 3) * 8;
        auto stage = [&](int blk) {
#pragma unroll
            for (int i = 0; i < 8; ++i) dma16(lb + (blk % 5) * 8192 + i * 1024, vo, blk < 8 ? s1 : s2, (unsigned)(((blk < 8 ? (8 * w + blk) : w) * 128 + i * 16) * 1024));
        };
#pragma unroll
        for (int blk = 0; blk < 5; ++blk) { stage(blk); if (0) __builtin_amdgcn_s_sleep(0); }
#pragma unroll
        for (int blk = 0; blk < 9; ++blk) {
            if (blk <= 4) asm volatile("s_waitcnt vmcnt(32)" ::: "memory"); else if (blk == 5) asm volatile("s_waitcnt vmcnt(24)" ::: "memory");
            else if (blk == 6) asm volatile("s_waitcnt vmcnt(16)" ::: "memory"); else if (blk == 7) asm volatile("s_waitcnt vmcnt(8)" ::: "memory"); else asm volatile("s_waitcnt vmcnt(0)" ::: "memory");
#pragma unroll
            for (int ks = 0; ks < 4; ++ks)
#pragma unroll
                for (int c2 = 0; c2 < 2; ++c2) { const v2u a0 = trread(b + (blk % 5) * 8192 + ks * 2048 + c2 * 32), a1 = trread(b + (blk % 5) * 8192 + ks * 2048 + 1024 + c2 * 32); wof[blk][ks][c2] = (v4u){a0.x, a0.y, a1.x, a1.y}; }
            if (blk + 5 < 9) {
#pragma unroll
                for (int ks = 0; ks < 4; ++ks) asm volatile("" : "+v"(wof[blk][ks][0]), "+v"(wof[blk][ks][1]));
                stage(blk + 5); if (0) __builtin_amdgcn_s_sleep(0);
            }
        }
    }
    {   bool ok = false;
        for (unsigned sp = 0; sp < SPIN; ++sp) {
            asm volatile("" ::: "memory");
            const v2u f = ldg8(ws, WS_BPROG + lane * 64), f1 = ldg8(ws, WS_BPROG + min(lane + 64, NB2 * 4 - 1) * 64), f2 = ldg8(ws, WS_PPROG + (lane & 31) * 128);
            if (__all(f.y == tag && f1.y == tag && f2.y == tag)) { ok = true; break; }
            __builtin_amdgcn_s_sleep(8);
        }
        asm volatile("" ::: "memory");
        if (!ok) { stg4(0x606, ws, WS_ABORT); return; } }
    BARRIER();
    TR(4, RT());
    unsigned par = 0, front = 0;
    static_assert(NEARLY % 4 == 0, "");
    v4u obf[4][2]; unsigned gr[2]; v2u rf; int pre = -1;
    auto request = [&](int s) {
#pragma unroll
        for (int qt = 0; qt < 2; ++qt) gr[qt] = ldg4(ws, WS_GBUF + (s * 32 + qt * 16 + c) * 4);
#pragma unroll
        for (int ks = 0; ks < 4; ++ks)
#pragma unroll
            for (int qt = 0; qt < 2; ++qt) { const unsigned o = WS_OB + (((8 * w + 2 * ks) * 128 + 2 * s + qt) * 64 + lane) * 8; const v2u a = ldg8(ws, o), b = ldg8(ws, o + 65536); obf[ks][qt] = (v4u){a.x, a.y, b.x, b.y}; }
        rf = ldg8(ws, WS_TPROG + ((s & 3) * 32 + 8 * w + (lane & 7)) * 64);
        pre = s;
    };
    SEGDECL; TR(26, RT()); TR(27, CKV());
    auto item = [&](int s, int snext, auto QMc) -> bool {
        constexpr int QM = decltype(QMc)::value, NQ = QM == 3 ? 2 : 1, QB = QM == 2 ? 1 : 0, NF = 32 * NQ;
        v4f acc[2][NQ], accB[2][NQ] = {};
        float gq[NQ];
        if (QM != 3 || pre != s) {
            for (unsigned sp = 0; front < 8u * (s + 1); ++sp) {
                asm volatile("" ::: "memory");
                const int w2 = min(lane + 64, NB2 * 4 - 1);
                const v2u f = ldg8(ws, WS_BPROG + lane * 64), f2 = ldg8(ws, WS_BPROG + w2 * 64);
                unsigned v = min(((lane >> 4) * 4 + (lane & 3)) + NB2 * (f.y == tag ? f.x : 0u), ((w2 >> 4) * 4 + (w2 & 3)) + NB2 * (f2.y == tag ? f2.x : 0u));
#pragma unroll
                for (int m = 1; m < 64; m <<= 1) v = min(v, (unsigned)__shfl_xor((int)v, m));
                front = rfl(v);
                if (front >= 8u * (s + 1)) break;
                if (sp > SPIN) { stg4(0x608, ws, WS_ABORT); return false; }
                __builtin_amdgcn_s_sleep(8);
            }
            asm volatile("" ::: "memory");
            request(s);
        }
        const unsigned ob = WS_OX + (s * 32 + 8 * w) * 8192 + QB * 1024 + lane * 16;
        auto ready = [&]() -> bool {
            if (QM == 3 || s < 62) { const v2u f = ldg8(ws, WS_TPROG + ((s & 3) * 32 + 8 * w + (lane & 7)) * 64); return __all(f.y == tag && f.x > (unsigned)(s >> 2)); }
            return !__any(ldg4(ws, WS_OX + (s * 32 + 8 * w + (lane >> 3)) * 8192 + 6144 + QB * 1024 + (lane & 7) * 128) == SENT);
        };
        bool ok = (QM == 3 || s < 62) && __all(rf.y == tag && rf.x > (unsigned)(s >> 2));
#pragma unroll
        for (int qt = 0; qt < NQ; ++qt) gq[qt] = fb(gr[QB + qt]);
#pragma unroll
        for (int ks = 0; ks < 4; ++ks)
#pragma unroll
            for (int qt = 0; qt < NQ; ++qt) { accB[0][qt] = mfma32(wof[8][ks][0], obf[ks][QB + qt], accB[0][qt]); accB[1][qt] = mfma32(wof[8][ks][1], obf[ks][QB + qt], accB[1][qt]); }
#pragma unroll
        for (int qt = 0; qt < NQ; ++qt) { accB[0][qt] *= 1.0f - gq[qt]; accB[1][qt] *= 1.0f - gq[qt]; asm volatile("" : "+v"(accB[0][qt]), "+v"(accB[1][qt]), "+v"(gq[qt])); }
        bool lastok = false;
        for (unsigned sp = 0;; ++sp) {
            asm volatile("" ::: "memory");
            if (sp) ok = ready();
            if (ok) {
                asm volatile("" ::: "memory");
                if (QM != 3) TR(32, RT());
                SEG(0);
                constexpr int WIN = NF < 28 ? NF : 28, ST = 2048 / NQ;
                v4u of[NF];
#pragma unroll
                for (int qt = 0; qt < NQ; ++qt) acc[0][qt] = acc[1][qt] = (v4f){};
#pragma unroll
                for (int i = 0; i < WIN; ++i) of[i] = ldg16(ws, ob + i * ST);
#pragma unroll
                for (int i = 0; i < NF; ++i) {
                    if (i + WIN < NF) of[i + WIN] = ldg16(ws, ob + (i + WIN) * ST);
                    const int u = i / NQ;
                    __builtin_amdgcn_sched_barrier(0);
                    acc[0][i % NQ] = mfma32(wof[u >> 2][u & 3][0], of[i], acc[0][i % NQ]);
                    acc[1][i % NQ] = mfma32(wof[u >> 2][u & 3][1], of[i], acc[1][i % NQ]);
                    __builtin_amdgcn_sched_barrier(0);
                }
                if (QM == 3 || s < 62) break;
                if (!__any(acc[0][0][0] != acc[0][0][0])) break;
                bool miss = false;
                for (int i = 0; i < NF; ++i) { const v4u x = ldg16(ws, ob + i * ST); miss |= x.x == SENT || x.y == SENT || x.z == SENT || x.w == SENT; }
                if (!__any(miss) && lastok) break;
                lastok = !__any(miss);
            }
            if (sp > SPIN) { stg4(0x700 + s, ws, WS_ABORT); return false; }
            if (QM == 3) __builtin_amdgcn_s_sleep(1);
        }
        if (QM != 3) TR(33, RT());
        SEG(1);
        if (QM == 3) { request(snext); if (front < 8u * (snext + 1)) pre = -1; }
        LAS unsigned char* rb = lds + (par ^= 16384);
#pragma unroll
        for (int c2 = 0; c2 < 2; ++c2)
#pragma unroll
            for (int qt = 0; qt < NQ; ++qt) *(LAS v4f*)(rb + (w * 4 + c2 * 2 + QB + qt) * 1024 + lane * 16) = acc[c2][qt] * gq[qt] + accB[c2][qt];
        BARRIER();
        if (QM == 3 || (w & 1) == QB) {
            const LAS v4f* q = (const LAS v4f*)(rb + w * 1024 + lane * 16);
            const v4f z = (q[0] + q[256]) + (q[512] + q[768]);
            *(GAS v2u*)((GAS unsigned char*)p.out + (size_t)(s * 32 + (w & 1) * 16 + c) * 1024 + ct * 64 + (w >> 1) * 32 + g * 8) = (v2u){pk(z.x, z.y), pk(z.z, z.w)};
        }
        if (QM != 3) TR(34, RT());
        SEG(2);
        TR(64 + s, RT());
        return true;
    };
    constexpr int NHALF = 55;
    const int sh = NHALF + grp % 3;
    for (int s = grp < 4 ? grp : NEARLY + grp; s < NHALF;) {
        const int sn = s + 4 < NEARLY ? s + 4 : (s < NEARLY ? NEARLY + grp : s + NGRP);
        if (!item(s, sn, IC<3>{})) return;
        s = sn;
    }
    SEGOUT(16); TR(24, RT()); TR(25, CKV());
    for (int s = sh; s < 64; s += 3) {
        if (!(grp >= 3 ? item(s, -1, IC<2>{}) : item(s, -1, IC<1>{}))) return;
    }
    TR(2, RT());
    if (grp == 5 && ct == 0 && t == 0) stg4(tag, ws, WS_E);
}

__device__ __forceinline__ Params kargs() {
    auto k = (const __attribute__((address_space(4))) Params*)__builtin_amdgcn_kernarg_segment_ptr();
    asm volatile("" : "+s"(k));
    Params q;
    q.x = k->x; q.mem0 = k->mem0; q.wq = k->wq; q.wk = k->wk; q.wv = k->wv; q.wo = k->wo; q.bqkv = k->bqkv; q.bwo = k->bwo; q.gate = k->gate; q.out = k->out; q.ws = k->ws;
    return q;
}
__global__ void __launch_bounds__(256, 1) gated_mix_mega(Params p0) {
    extern __shared__ __attribute__((aligned(16))) unsigned char smem[];
    LAS unsigned char* lds = (LAS unsigned char*)smem;
    const int b = blockIdx.x, w = __builtin_amdgcn_readfirstlane(threadIdx.x >> 6);
    const int idx = (b >> 3) * 4 + (b & 3) + ((b & 4) ? 128 : 0);
    if (idx < 64) { role_head(kargs(), ((idx >> 3) << 2) | (idx & 3), (idx >> 2) & 1, w); return; }
    const rsrc_t ws = __builtin_amdgcn_make_buffer_rsrc(p0.ws, 0, WS_END, 0x00020000);
    TR(0, RT());
    if (idx < 128) { role_reducer(kargs(), idx - 64, w, lds, ws); return; }
    unsigned tag;
    const int f = idx - 128;
    if (f < 96) {
        if (f < 64 && (f & 4)) role_P<2>(kargs(), ((f >> 3) << 2) | (f & 3), 2, w, lds, ws, tag);
        else role_P<0>(kargs(), f < 64 ? ((f >> 3) << 2) | (f & 3) : f - 64, f < 64 ? 1 : 0, w, lds, ws, tag);
    } else if (f < 104) role_P<3>(kargs(), f - 96, 0, w, lds, ws, tag);
    else if (f < 108) role_P<4>(kargs(), f - 96, 0, w, lds, ws, tag);
    else role_B2(kargs(), f - 108, w, ws, tag);
    TR(1, RT());
    const int x = f & 3, y = f >> 2;
    if (y < 8) role_T(kargs(), x * 8 + y, w, ws, tag);
    else if (y < 24) role_W(kargs(), x, y - 8, w, lds, ws, tag);
    else role_W(kargs(), 4 + (x >> 1), (x & 1) * 8 + (y - 24), w, lds, ws, tag);
}

extern "C" void kernel_launch(void* const* d_in, const int* in_sizes, int n_in, void* d_out, int out_size, void* d_ws, size_t ws_size, hipStream_t stream) {
    Params p;
    p.x = d_in[0]; p.mem0 = d_in[1]; p.wq = d_in[2]; p.wk = d_in[3]; p.wv = d_in[4]; p.wo = d_in[5];
    p.bqkv = d_in[8]; p.bwo = d_in[9]; p.gate = d_in[10]; p.out = d_out;
    p.ws = (unsigned char*)(((uintptr_t)d_ws + (2u << 20) - 1) & ~(uintptr_t)((2u << 20) - 1));
    (void)hipFuncSetAttribute((const void*)gated_mix_mega, hipFuncAttributeMaxDynamicSharedMemorySize, LDS_BYTES);
    hipLaunchKernelGGL(gated_mix_mega, dim3(256), dim3(256), LDS_BYTES, stream, p);
}
```
